```python
import torch

def _softmax(x, axis=-1):
    return torch.softmax(x, dim=axis)
def _gelu(x):
    return torch.nn.functional.gelu(x, approximate="tanh")
def _silu(x):
    return torch.nn.functional.silu(x)
def _sigmoid(x):
    return torch.sigmoid(x)
def _layernorm(x, axis=-1, eps=1e-5):
    m = x.mean(dim=axis, keepdim=True)
    v = x.var(dim=axis, keepdim=True, unbiased=False)
    return (x - m) / torch.sqrt(v + eps)
def _scatter_add(tgt, idx, src, axis=0):
    return tgt.clone().index_add_(axis, idx, src)
def _segment_sum(src, seg, n):
    out = torch.zeros((n,) + src.shape[1:], dtype=src.dtype)
    return out.index_add_(0, seg, src)
def _pad(x, pw):
    flat = tuple(v for pair in reversed(pw) for v in pair)
    return torch.nn.functional.pad(x, flat)

kernel_name = 'standalone__glu_gated_glu__prompt'

def setup_inputs():

    _w = (lambda a: (lambda t: t.to(torch.bfloat16) if t.is_floating_point() else t)(torch.as_tensor(a)))
    B, S, D, F = 1, 2048, 640, 1536
    return {
        "x": _w((lambda *s: torch.randn(*s, dtype=torch.bfloat16) * 0.2)(B, S, D)),
        "w_gate": _w((lambda *s: torch.randn(*s, dtype=torch.bfloat16) * 0.2)(D, F)),
        "w_up": _w((lambda *s: torch.randn(*s, dtype=torch.bfloat16) * 0.05)(D, F)),
        "w_down": _w((lambda *s: torch.randn(*s, dtype=torch.bfloat16) * 0.05)(F, D)),
    }

def reference(x, w_gate, w_up, w_down):
    g = torch.einsum("bsd,df->bsf", x, w_gate)
    u = torch.einsum("bsd,df->bsf", x, w_up)
    h = _sigmoid(g) * u
    return torch.einsum("bsf,fd->bsd", h, w_down)
```

```cpp
#include <hip/hip_runtime.h>
#include <hip/hip_bf16.h>
typedef __attribute__((ext_vector_type(4))) short s16x4;
typedef __attribute__((ext_vector_type(8))) short s16x8;
typedef __attribute__((ext_vector_type(8))) __bf16 bf16x8;
typedef __attribute__((ext_vector_type(2))) __bf16 bf16x2;
typedef __attribute__((ext_vector_type(4))) float f32x4;
typedef __attribute__((ext_vector_type(2))) float f32x2;
typedef __attribute__((ext_vector_type(16))) float f32x16;
typedef __attribute__((ext_vector_type(4))) unsigned u32x4;
typedef __attribute__((ext_vector_type(2))) unsigned u32x2;
#define LAS __attribute__((address_space(3)))
#define GAS __attribute__((address_space(1)))

constexpr int M = 2048, D = 640, F = 1536;

template<int N> __device__ __forceinline__ void waitv(){ asm volatile("s_waitcnt vmcnt(%0)"::"i"(N):"memory"); }
__device__ __forceinline__ void glds16(const void* g, void* l){ __builtin_amdgcn_global_load_lds((const GAS void*)g, (LAS void*)l, 16, 0, 0); }
__device__ __forceinline__ s16x4 trread(const char* l){ return __builtin_amdgcn_ds_read_tr16_b64_v4i16((LAS s16x4*)l); }
__device__ __forceinline__ bf16x8 cat(s16x4 a, s16x4 b){ s16x8 v = __builtin_shufflevector(a, b, 0,1,2,3,4,5,6,7); return __builtin_bit_cast(bf16x8, v); }
__device__ __forceinline__ unsigned pk(float a, float b){ f32x2 v = {a, b}; bf16x2 r = __builtin_convertvector(v, bf16x2); return __builtin_bit_cast(unsigned, r); }

constexpr int STG = 40960 + 64;
constexpr int OFF_G = 16384, OFF_U = 16384 + 12288 + 32;
constexpr int NST = 3;

__global__ __launch_bounds__(256) void k1(const char* __restrict__ X, const char* __restrict__ Wg, const char* __restrict__ Wu, char* __restrict__ H){
  extern __shared__ __attribute__((aligned(16))) char smem[];
  const int bid = blockIdx.x, xcd = bid & 7, idx = bid >> 3;
  const int mt = (xcd & 1) * 8 + (idx & 7), ft = (xcd >> 1) * 4 + (idx >> 3);
  const int tid = threadIdx.x, lane = tid & 63, w = __builtin_amdgcn_readfirstlane(tid >> 6);
  const int wm = w & 1, wf = w >> 1;
  const char* sx[4]; const char* sg[3]; const char* su[3];
  #pragma unroll
  for (int i = 0; i < 4; ++i) { int r = (w * 4 + i) * 8 + (lane >> 3), p = lane & 7; sx[i] = X + (size_t)(mt * 128 + r) * 1280 + ((p ^ ((r >> 1) & 7)) * 16); }
  #pragma unroll
  for (int i = 0; i < 3; ++i) { int c = (w * 3 + i) * 64 + lane, r = c / 12, ch = c % 12; size_t o = (size_t)r * 3072 + ft * 192 + ch * 16; sg[i] = Wg + o; su[i] = Wu + o; }
  auto stage = [&](int s, int buf) {
    char* b = smem + buf * STG;
    #pragma unroll
    for (int i = 0; i < 4; ++i) glds16(sx[i] + s * 128, b + (w * 4 + i) * 1024);
    #pragma unroll
    for (int i = 0; i < 3; ++i) glds16(sg[i] + (size_t)s * 64 * 3072, b + OFF_G + (w * 3 + i) * 1024);
    #pragma unroll
    for (int i = 0; i < 3; ++i) glds16(su[i] + (size_t)s * 64 * 3072, b + OFF_U + (w * 3 + i) * 1024);
  };
  f32x16 acc[2][3];
  #pragma unroll
  for (int a = 0; a < 2; ++a) for (int b = 0; b < 3; ++b) for (int r = 0; r < 16; ++r) acc[a][b][r] = 0.f;
  const int h = lane >> 5;
  int xoff[2][4];
  #pragma unroll
  for (int a = 0; a < 2; ++a) { int r = wm * 64 + a * 32 + (lane & 31);
    #pragma unroll
    for (int ks = 0; ks < 4; ++ks) xoff[a][ks] = r * 128 + (((2 * ks + h) ^ ((r >> 1) & 7)) * 16); }
  const int woff = (((lane >> 4) & 1) ? OFF_U : OFF_G) + (8 * h + ((lane & 15) >> 2)) * 192 + wf * 96 + (lane & 3) * 8;
  stage(0, 0); stage(1, 1);
  #pragma unroll
  for (int s = 0; s < 10; ++s) {
    if (s < 9) waitv<10>(); else waitv<0>();
    __builtin_amdgcn_s_barrier();
    if (s + 2 < 10) stage(s + 2, (s + 2) % NST);
    const char* b = smem + (s % NST) * STG;
    #pragma unroll
    for (int ks = 0; ks < 4; ++ks) {
      bf16x8 xf[2], wfr[3];
      #pragma unroll
      for (int a = 0; a < 2; ++a) xf[a] = *(const bf16x8*)(b + xoff[a][ks]);
      #pragma unroll
      for (int j = 0; j < 3; ++j) { const char* p = b + woff + ks * 16 * 192 + j * 32; wfr[j] = cat(trread(p), trread(p + 4 * 192)); }
      #pragma unroll
      for (int a = 0; a < 2; ++a)
        #pragma unroll
        for (int j = 0; j < 3; ++j) acc[a][j] = __builtin_amdgcn_mfma_f32_32x32x16_bf16(wfr[j], xf[a], acc[a][j], 0, 0, 0);
    }
    asm volatile("s_waitcnt lgkmcnt(0)" ::: "memory");
  }
  #pragma unroll
  for (int a = 0; a < 2; ++a)
    #pragma unroll
    for (int j = 0; j < 3; ++j) {
      float hv[8];
      #pragma unroll
      for (int r = 0; r < 8; ++r) { float g = acc[a][j][r], u = acc[a][j][r + 8]; hv[r] = u * __builtin_amdgcn_rcpf(1.f + __builtin_amdgcn_exp2f(-1.44269504088896f * g)); }
      u32x4 o = {pk(hv[0], hv[1]), pk(hv[2], hv[3]), pk(hv[4], hv[5]), pk(hv[6], hv[7])};
      int m = mt * 128 + wm * 64 + a * 32 + (lane & 31), fb = ft * 6 + wf * 3 + j;
      size_t off = ((((size_t)(m >> 4) * 48 + (fb >> 1)) * 4 + (fb & 1) * 2 + h) * 16 + (m & 15)) * 16;
      *(u32x4*)(H + off) = o;
    }
}

__global__ __launch_bounds__(256) void k2(const char* __restrict__ H, const char* __restrict__ Wd, char* __restrict__ out){
  extern __shared__ __attribute__((aligned(16))) char smem[];
  const int bid = blockIdx.x, xcd = bid & 7, idx = bid >> 3;
  const int mt = xcd * 4 + (idx & 3), nt = idx >> 2;
  const int tid = threadIdx.x, lane = tid & 63, w = __builtin_amdgcn_readfirstlane(tid >> 6);
  char* my = smem + w * 30720;
  const int g = lane >> 4;
  f32x4 acc[4][5];
  #pragma unroll
  for (int a = 0; a < 4; ++a) for (int b = 0; b < 5; ++b) acc[a][b] = f32x4{0.f, 0.f, 0.f, 0.f};
  const int woff = (16 * (g >> 1) + 4 * (g & 1) + ((lane & 15) >> 2)) * 160 + (lane & 3) * 8;
  #pragma unroll
  for (int half = 0; half < 2; ++half) {
    const int f0 = w * 384 + half * 192;
    #pragma unroll
    for (int i = 0; i < 30; ++i) { int c = i * 64 + lane, r = c / 10, ch = c % 10; glds16(Wd + (size_t)(f0 + r) * 1280 + nt * 160 + ch * 16, my + i * 1024); }
    bf16x8 hf[6][4];
    #pragma unroll
    for (int s = 0; s < 6; ++s)
      #pragma unroll
      for (int a = 0; a < 4; ++a) hf[s][a] = *(const bf16x8*)(H + (((size_t)(mt * 4 + a) * 48 + (f0 >> 5) + s) * 64 + lane) * 16);
    waitv<0>();
    #pragma unroll
    for (int s = 0; s < 6; ++s) {
      bf16x8 wfr[5];
      #pragma unroll
      for (int j = 0; j < 5; ++j) { const char* p = my + woff + s * 32 * 160 + j * 32; wfr[j] = cat(trread(p), trread(p + 8 * 160)); }
      #pragma unroll
      for (int a = 0; a < 4; ++a)
        #pragma unroll
        for (int j = 0; j < 5; ++j) acc[a][j] = __builtin_amdgcn_mfma_f32_16x16x32_bf16(wfr[j], hf[s][a], acc[a][j], 0, 0, 0);
    }
    asm volatile("s_waitcnt lgkmcnt(0)" ::: "memory");
  }
  __syncthreads();
  #pragma unroll
  for (int a = 0; a < 4; ++a)
    if (a != w) {
      #pragma unroll
      for (int j = 0; j < 5; ++j) *(f32x4*)(smem + (((a * 4 + w) * 5 + j) * 64 + lane) * 16) = acc[a][j];
    }
  __syncthreads();
  f32x4 r[5];
  #pragma unroll
  for (int j = 0; j < 5; ++j) r[j] = w == 0 ? acc[0][j] : w == 1 ? acc[1][j] : w == 2 ? acc[2][j] : acc[3][j];
  #pragma unroll
  for (int sw = 0; sw < 4; ++sw)
    if (sw != w) {
      #pragma unroll
      for (int j = 0; j < 5; ++j) r[j] += *(const f32x4*)(smem + (((w * 4 + sw) * 5 + j) * 64 + lane) * 16);
    }
  const int m = mt * 64 + w * 16 + (lane & 15);
  #pragma unroll
  for (int j = 0; j < 5; ++j) { u32x2 o = {pk(r[j][0], r[j][1]), pk(r[j][2], r[j][3])}; *(u32x2*)(out + (size_t)m * 1280 + (nt * 80 + j * 16 + g * 4) * 2) = o; }
}

extern "C" void kernel_launch(void* const* d_in, const int* in_sizes, int n_in, void* d_out, int out_size, void* d_ws, size_t ws_size, hipStream_t stream) {
  const char* X = (const char*)d_in[0]; const char* Wg = (const char*)d_in[1]; const char* Wu = (const char*)d_in[2]; const char* Wd = (const char*)d_in[3];
  char* H = (char*)d_ws;
  (void)hipFuncSetAttribute((const void*)k1, hipFuncAttributeMaxDynamicSharedMemorySize, NST * STG);
  (void)hipFuncSetAttribute((const void*)k2, hipFuncAttributeMaxDynamicSharedMemorySize, 4 * 30720);
  k1<<<256, 256, NST * STG, stream>>>(X, Wg, Wu, H);
  k2<<<256, 256, 4 * 30720, stream>>>(H, Wd, (char*)d_out);
}
```

```cpp
#include <hip/hip_runtime.h>

__global__ __launch_bounds__(512, 2) void glu_fused(const void* X, const void* Wg, const void* Wu, const void* Wd, void* out, void* ws) {
  __shared__ __attribute__((aligned(16))) char smem[163840];
  unsigned tid = threadIdx.x;
  asm volatile(
    "LBEG_%=:\n"
    "s_load_dwordx8 s[4:11], s[0:1], 0x0\n"
    "s_load_dwordx4 s[12:15], s[0:1], 0x20\n"
    "v_lshrrev_b32 v1, 6, v0\n"
    "v_and_b32 v0, 63, v0\n"
    "v_readfirstlane_b32 s44, v1\n"
    "s_and_b32 s45, s2, 7\n"
    "s_lshr_b32 s46, s2, 3\n"
    "s_mul_i32 s49, s46, 52\n"
    "s_lshr_b32 s49, s49, 8\n"
    "s_and_b32 s55, s45, 1\n"
    "s_sub_u32 s55, s55, s49\n"
    "s_mul_i32 s55, s55, 5\n"
    "s_add_u32 s47, s55, s46\n"
    "s_cmp_ge_u32 s46, 30\n"
    "s_cselect_b32 s47, 10, s47\n"
    "s_cselect_b32 s56, 32, 48\n"
    "s_mul_i32 s47, s47, 192\n"
    "s_cmp_ge_u32 s44, 4\n"
    "s_cbranch_scc1 LC_%=\n"
    "s_getpc_b64 s[54:55]\n"
    "Lpc1_%=:\n"
    "s_sub_u32 s54, s54, Lpc1_%=-LBEG_%=\n"
    "s_subb_u32 s55, s55, 0\n"
    "v_and_b32 v16, 1, v0\n"
    "v_lshlrev_b32 v16, 7, v16\n"
    "s_lshl2_add_u32 s57, s46, s44\n"
    "s_lshl_b32 s57, s57, 8\n"
    "v_add_u32 v16, s57, v16\n"
    "v_min_u32 v16, LEND_%=-LBEG_%=, v16\n"
    "global_load_dword v19, v16, s[54:55]\n"
    "s_mov_b32 s54, 69338144\n"
    "s_mov_b32 s55, 4326466\n"
    "s_cmp_ge_u32 s46, 16\n"
    "s_cselect_b32 s54, 541204546, s54\n"
    "s_cselect_b32 s55, 1141121092, s55\n"
    "s_lshl_b32 s57, s46, 2\n"
    "s_lshr_b64 s[54:55], s[54:55], s57\n"
    "s_and_b32 s54, s54, 15\n"
    "s_lshl_b32 s55, s45, 2\n"
    "s_lshr_b32 s55, 375457104, s55\n"
    "s_and_b32 s55, s55, 15\n"
    "s_add_u32 s60, s54, s55\n"
    "s_mov_b32 s62, s55\n"
    "s_mov_b32 s63, s54\n"
    "s_sub_u32 s55, s60, 10\n"
    "s_cmp_ge_u32 s60, 10\n"
    "s_cselect_b32 s60, s55, s60\n"
    "s_lshl_b32 s58, s60, 7\n"
    "s_mul_i32 s54, s44, s56\n"
    "s_lshl_b32 s96, s54, 7\n"
    "s_add_u32 s54, s54, s47\n"
    "v_lshrrev_b32 v12, 3, v0\n"
    "v_and_b32 v13, 7, v0\n"
    "v_lshrrev_b32 v14, 4, v0\n"
    "v_add_u32 v16, s54, v12\n"
    "v_xor_b32 v15, v13, v14\n"
    "v_mul_u32_u24 v16, 1280, v16\n"
    "v_lshl_add_u32 v2, v15, 4, v16\n"
    "v_xor_b32 v9, 64, v2\n"
    "v_add_u32 v3, 9216, v9\n"
    "v_add_u32 v4, 18432, v2\n"
    "v_add_u32 v5, 27648, v9\n"
    "s_mov_b32 s18, 2621440\n"
    "s_mov_b32 s19, 0x20000\n"
    "s_waitcnt lgkmcnt(0)\n"
    "s_mov_b32 s16, s4\n"
    "s_and_b32 s17, s5, 0xffff\n"
    "s_add_u32 m0, s96, 0\n"
    "s_nop 0\n"
    "buffer_load_dwordx4 v2, s[16:19], s58 offen offset:0 lds\n"
    "buffer_load_dwordx4 v3, s[16:19], s58 offen offset:1024 lds\n"
    "buffer_load_dwordx4 v4, s[16:19], s58 offen offset:2048 lds\n"
    "buffer_load_dwordx4 v5, s[16:19], s58 offen offset:3072 lds\n"
    "s_mov_b32 s22, 1966080\n"
    "s_mov_b32 s23, 0x20000\n"
    "s_mov_b32 s26, 1966080\n"
    "s_mov_b32 s27, 0x20000\n"
    "s_mov_b32 s20, s6\n"
    "s_and_b32 s21, s7, 0xffff\n"
    "s_mov_b32 s24, s8\n"
    "s_and_b32 s25, s9, 0xffff\n"
    "s_lshr_b32 s57, s45, 1\n"
    "s_mul_i32 s48, s57, 6\n"
    "s_add_u32 s48, s48, s49\n"
    "s_lshl1_add_u32 s55, s45, s46\n"
    "s_sub_u32 s55, s55, 30\n"
    "s_cmp_ge_u32 s46, 30\n"
    "s_cselect_b32 s48, s55, s48\n"
    "s_cselect_b64 s[32:33], s[20:21], s[16:17]\n"
    "s_cselect_b64 s[36:37], s[24:25], s[16:17]\n"
    "s_cselect_b64 s[34:35], s[22:23], s[18:19]\n"
    "s_cselect_b64 s[38:39], s[26:27], s[18:19]\n"
    "s_cbranch_scc1 LLodd_%=\n"
    "v_add_u32 v8, 40960, v2\n"
    "v_add_u32 v9, 51200, v9\n"
    "v_and_b32 v14, 1, v14\n"
    "v_lshlrev_b32 v14, 1, v14\n"
    "v_xor_b32 v14, v14, v13\n"
    "s_lshl_b32 s54, s44, 4\n"
    "v_add_u32 v16, s54, v12\n"
    "v_mul_u32_u24 v16, 3072, v16\n"
    "s_lshl_b32 s54, s48, 7\n"
    "v_lshl_add_u32 v16, v14, 4, v16\n"
    "v_add_u32 v6, s54, v16\n"
    "v_xor_b32 v7, 64, v6\n"
    "v_add_u32 v7, 23552, v7\n"
    "s_lshl_b32 s97, s44, 11\n"
    "s_add_u32 s50, s97, 32768\n"
    "s_add_u32 s97, s97, 24576\n"
    "s_add_u32 s51, s96, 4096\n"
    "s_add_u32 s52, s96, 5120\n"
    "s_mov_b32 s53, 128\n"
    "s_branch LLgo_%=\n"
    "LLodd_%=:\n"
    "s_mul_i32 s54, s44, 192\n"
    "s_mul_i32 s55, s48, 192\n"
    "v_add_u32 v16, s54, v0\n"
    "v_mul_u32_u24 v17, 43691, v16\n"
    "v_lshrrev_b32 v17, 19, v17\n"
    "v_mul_u32_u24 v17, 2880, v17\n"
    "v_lshl_add_u32 v16, v16, 4, v17\n"
    "v_add_u32 v6, s55, v16\n"
    "v_add_u32 v16, s54, v0\n"
    "v_add_u32 v16, 64, v16\n"
    "v_mul_u32_u24 v17, 43691, v16\n"
    "v_lshrrev_b32 v17, 19, v17\n"
    "v_mul_u32_u24 v17, 2880, v17\n"
    "v_lshl_add_u32 v16, v16, 4, v17\n"
    "v_add_u32 v7, s55, v16\n"
    "v_add_u32 v16, s54, v0\n"
    "v_add_u32 v16, 128, v16\n"
    "v_mul_u32_u24 v17, 43691, v16\n"
    "v_lshrrev_b32 v17, 19, v17\n"
    "v_mul_u32_u24 v17, 2880, v17\n"
    "v_lshl_add_u32 v16, v16, 4, v17\n"
    "v_add_u32 v8, s55, v16\n"
    "v_subrev_u32 v7, 1024, v7\n"
    "v_mov_b32 v9, v8\n"
    "s_mul_i32 s97, s44, 3072\n"
    "s_add_u32 s50, s97, 28688\n"
    "s_add_u32 s97, s97, 16384\n"
    "s_add_u32 s51, s97, 2048\n"
    "s_add_u32 s52, s50, 2048\n"
    "s_mov_b32 s53, 196608\n"
    "LLgo_%=:\n"
    "s_mul_i32 s59, s60, 196608\n"
    "s_mul_i32 s61, s60, s53\n"
    "s_add_u32 m0, s51, 0\n"
    "s_nop 0\n"
    "buffer_load_dwordx4 v8, s[32:35], s61 offen offset:0 lds\n"
    "s_add_u32 m0, s52, 0\n"
    "s_nop 0\n"
    "buffer_load_dwordx4 v9, s[36:39], s61 offen offset:0 lds\n"
    "s_add_u32 m0, s97, 0\n"
    "s_nop 0\n"
    "buffer_load_dwordx4 v6, s[20:23], s59 offen offset:0 lds\n"
    "buffer_load_dwordx4 v7, s[20:23], s59 offen offset:1024 lds\n"
    "s_add_u32 m0, s50, 0\n"
    "s_nop 0\n"
    "buffer_load_dwordx4 v6, s[24:27], s59 offen offset:0 lds\n"
    "buffer_load_dwordx4 v7, s[24:27], s59 offen offset:1024 lds\n"
    "s_mov_b32 s47, s62\n"
    "s_mov_b32 s12, 1985229328\n"
    "s_mov_b32 s13, 152\n"
    "s_cmp_eq_u32 s63, 2\n"
    "s_cselect_b32 s12, 2557543474, s12\n"
    "s_cselect_b32 s13, 101, s13\n"
    "s_cmp_eq_u32 s63, 4\n"
    "s_cselect_b32 s12, 1704141060, s12\n"
    "s_cselect_b32 s13, 135, s13\n"
    "s_load_dwordx2 s[62:63], s[14:15], 0x0\n"
    "s_mov_b32 s28, s10\n"
    "s_and_b32 s29, s11, 0xffff\n"
    "s_mul_i32 s54, s46, 61440\n"
    "s_lshr_b32 s55, s45, 1\n"
    "s_lshl_b32 s55, s55, 3\n"
    "s_lshr_b32 s55, 319752963, s55\n"
    "s_and_b32 s55, s55, 0xff\n"
    "s_lshl_b32 s55, s55, 7\n"
    "s_add_u32 s54, s54, s55\n"
    "s_cmp_eq_u32 s55, 1920\n"
    "s_cselect_b32 s56, 0, 19\n"
    "s_lshl1_add_u32 s55, s46, s44\n"
    "s_mul_i32 s55, s55, 2560\n"
    "s_add_u32 s55, s55, 2452480\n"
    "v_min_u32 v17, 19, v0\n"
    "v_lshlrev_b32 v17, 7, v17\n"
    "v_min_u32 v16, s56, v0\n"
    "v_mul_u32_u24 v16, 3072, v16\n"
    "s_cmp_ge_u32 s44, 2\n"
    "s_cbranch_scc1 Lbr4_%=\n"
    "s_cmp_eq_u32 s44, 1\n"
    "s_cbranch_scc1 Lbp2_%=\n"
    "buffer_load_dword v68, v16, s[20:23], s54 offen\n"
    "s_branch Lbq3_%=\n"
    "Lbp2_%=:\n"
    "buffer_load_dword v68, v16, s[24:27], s54 offen\n"
    "s_branch Lbq3_%=\n"
    "Lbr4_%=:\n"
    "buffer_load_dword v68, v17, s[16:19], s55 offen\n"
    "Lbq3_%=:\n"
    "s_mov_b32 s30, 1966080\n"
    "s_mov_b32 s31, 0x20000\n"
    "s_cmp_ge_u32 s46, 30\n"
    "s_cselect_b32 s30, 0, s30\n"
    "s_mul_i32 s54, s45, 30\n"
    "s_add_u32 s54, s54, s46\n"
    "s_mul_i32 s56, s54, 205\n"
    "s_lshr_b32 s56, s56, 10\n"
    "s_mul_i32 s55, s56, 5\n"
    "s_sub_u32 s57, s54, s55\n"
    "s_lshl2_add_u32 s54, s56, s44\n"
    "s_mul_i32 s54, s54, 10240\n"
    "s_lshl_b32 s55, s57, 8\n"
    "s_add_u32 s54, s54, s55\n"
    "s_cmp_ge_u32 s46, 30\n"
    "s_cselect_b32 s54, 0, s54\n"
    "s_add_u32 s55, s54, 4096\n"
    "v_lshrrev_b32 v18, 4, v0\n"
    "v_mul_u32_u24 v18, 1280, v18\n"
    "v_and_b32 v17, 15, v0\n"
    "v_lshl_add_u32 v18, v17, 4, v18\n"
    "s_lshl_b32 s56, s44, 11\n"
    "s_add_u32 m0, s56, 144256\n"
    "s_nop 0\n"
    "buffer_load_dwordx4 v18, s[28:31], s54 offen lds\n"
    "buffer_load_dwordx4 v18, s[28:31], s55 offen offset:1024 lds\n"
    "s_mov_b32 s42, 1048576\n"
    "s_mov_b32 s43, 0x20000\n"
    "s_mov_b32 s40, s14\n"
    "s_and_b32 s41, s15, 0xffff\n"
    "s_add_u32 s4, s14, 8388608\n"
    "s_addc_u32 s5, s15, 0\n"
    "s_and_b32 s5, s5, 0xffff\n"
    "s_mov_b32 s6, 1966080\n"
    "s_mov_b32 s7, 0x20000\n"
    "v_min_u32 v16, 1, v0\n"
    "s_mov_b32 m0, 144000\n"
    "v_lshlrev_b32 v16, 2, v16\n"
    "buffer_load_dword v16, s[40:43], 0 offen lds\n"
    "s_lshr_b64 s[12:13], s[12:13], 4\n"
    "s_and_b32 s60, s12, 15\n"
    "s_add_u32 s60, s60, s47\n"
    "s_sub_u32 s58, s60, 10\n"
    "s_cmp_ge_u32 s60, 10\n"
    "s_cselect_b32 s60, s58, s60\n"
    "s_lshl_b32 s58, s60, 7\n"
    "s_mul_i32 s59, s60, 196608\n"
    "s_mul_i32 s61, s60, s53\n"
    "s_add_u32 m0, s96, 41088\n"
    "s_nop 0\n"
    "buffer_load_dwordx4 v2, s[16:19], s58 offen offset:0 lds\n"
    "buffer_load_dwordx4 v3, s[16:19], s58 offen offset:1024 lds\n"
    "buffer_load_dwordx4 v4, s[16:19], s58 offen offset:2048 lds\n"
    "buffer_load_dwordx4 v5, s[16:19], s58 offen offset:3072 lds\n"
    "s_add_u32 m0, s51, 41088\n"
    "s_nop 0\n"
    "buffer_load_dwordx4 v8, s[32:35], s61 offen offset:0 lds\n"
    "s_add_u32 m0, s52, 41088\n"
    "s_nop 0\n"
    "buffer_load_dwordx4 v9, s[36:39], s61 offen offset:0 lds\n"
    "s_add_u32 m0, s97, 41088\n"
    "s_nop 0\n"
    "buffer_load_dwordx4 v6, s[20:23], s59 offen offset:0 lds\n"
    "v_and_b32 v20, 3, v0\n"
    "v_lshlrev_b32 v20, 2, v20\n"
    "v_add_u32 v20, 143744, v20\n"
    "s_lshl2_add_u32 s8, s44, 143744\n"
    "v_mov_b32 v21, s8\n"
    "v_lshlrev_b32 v23, 4, v0\n"
    "v_add_u32 v25, 4096, v23\n"
    "s_waitcnt vmcnt(11)\n"
    "s_barrier\n"
    "buffer_load_dwordx4 v7, s[20:23], s59 offen offset:1024 lds\n"
    "s_add_u32 m0, s50, 41088\n"
    "s_nop 0\n"
    "buffer_load_dwordx4 v6, s[24:27], s59 offen offset:0 lds\n"
    "buffer_load_dwordx4 v7, s[24:27], s59 offen offset:1024 lds\n"
    "s_lshr_b64 s[12:13], s[12:13], 4\n"
    "s_and_b32 s60, s12, 15\n"
    "s_add_u32 s60, s60, s47\n"
    "s_sub_u32 s58, s60, 10\n"
    "s_cmp_ge_u32 s60, 10\n"
    "s_cselect_b32 s60, s58, s60\n"
    "s_lshl_b32 s58, s60, 7\n"
    "s_mul_i32 s59, s60, 196608\n"
    "s_mul_i32 s61, s60, s53\n"
    "s_add_u32 m0, s96, 82176\n"
    "s_nop 0\n"
    "buffer_load_dwordx4 v2, s[16:19], s58 offen offset:0 lds\n"
    "buffer_load_dwordx4 v3, s[16:19], s58 offen offset:1024 lds\n"
    "buffer_load_dwordx4 v4, s[16:19], s58 offen offset:2048 lds\n"
    "buffer_load_dwordx4 v5, s[16:19], s58 offen offset:3072 lds\n"
    "s_add_u32 m0, s51, 82176\n"
    "s_nop 0\n"
    "buffer_load_dwordx4 v8, s[32:35], s61 offen offset:0 lds\n"
    "s_add_u32 m0, s52, 82176\n"
    "s_nop 0\n"
    "buffer_load_dwordx4 v9, s[36:39], s61 offen offset:0 lds\n"
    "s_add_u32 m0, s97, 82176\n"
    "s_nop 0\n"
    "buffer_load_dwordx4 v6, s[20:23], s59 offen offset:0 lds\n"
    "s_waitcnt vmcnt(7)\n"
    "s_barrier\n"
    "buffer_load_dwordx4 v7, s[20:23], s59 offen offset:1024 lds\n"
    "s_add_u32 m0, s50, 82176\n"
    "s_nop 0\n"
    "buffer_load_dwordx4 v6, s[24:27], s59 offen offset:0 lds\n"
    "buffer_load_dwordx4 v7, s[24:27], s59 offen offset:1024 lds\n"
    "s_lshr_b64 s[12:13], s[12:13], 4\n"
    "s_and_b32 s60, s12, 15\n"
    "s_add_u32 s60, s60, s47\n"
    "s_sub_u32 s58, s60, 10\n"
    "s_cmp_ge_u32 s60, 10\n"
    "s_cselect_b32 s60, s58, s60\n"
    "s_lshl_b32 s58, s60, 7\n"
    "s_mul_i32 s59, s60, 196608\n"
    "s_mul_i32 s61, s60, s53\n"
    "s_add_u32 m0, s96, 0\n"
    "s_nop 0\n"
    "buffer_load_dwordx4 v2, s[16:19], s58 offen offset:0 lds\n"
    "buffer_load_dwordx4 v3, s[16:19], s58 offen offset:1024 lds\n"
    "buffer_load_dwordx4 v4, s[16:19], s58 offen offset:2048 lds\n"
    "buffer_load_dwordx4 v5, s[16:19], s58 offen offset:3072 lds\n"
    "s_add_u32 m0, s51, 0\n"
    "s_nop 0\n"
    "buffer_load_dwordx4 v8, s[32:35], s61 offen offset:0 lds\n"
    "s_add_u32 m0, s52, 0\n"
    "s_nop 0\n"
    "buffer_load_dwordx4 v9, s[36:39], s61 offen offset:0 lds\n"
    "s_add_u32 m0, s97, 0\n"
    "s_nop 0\n"
    "buffer_load_dwordx4 v6, s[20:23], s59 offen offset:0 lds\n"
    "s_waitcnt vmcnt(7)\n"
    "s_waitcnt lgkmcnt(0)\n"
    "s_add_u32 s62, s62, 1\n"
    "s_addc_u32 s63, s63, 0\n"
    "s_barrier\n"
    "s_cmp_lg_u32 s44, 0\n"
    "s_cbranch_scc1 Lpk5_%=\n"
    "s_cmp_ge_u32 s46, 30\n"
    "s_cbranch_scc0 Lpk5_%=\n"
    "v_mov_b32 v16, s62\n"
    "v_mov_b32 v17, s63\n"
    "v_lshlrev_b32 v15, 3, v0\n"
    "s_mul_i32 s9, s2, 32\n"
    "s_add_u32 s9, s9, 4096\n"
    "s_mov_b64 exec, 15\n"
    "buffer_store_dwordx2 v[16:17], v15, s[40:43], s9 offen sc1\n"
    "s_mov_b64 exec, -1\n"
    "Lpk5_%=:\n"
    "buffer_load_dwordx4 v7, s[20:23], s59 offen offset:1024 lds\n"
    "s_add_u32 m0, s50, 0\n"
    "s_nop 0\n"
    "buffer_load_dwordx4 v6, s[24:27], s59 offen offset:0 lds\n"
    "buffer_load_dwordx4 v7, s[24:27], s59 offen offset:1024 lds\n"
    "s_lshr_b64 s[12:13], s[12:13], 4\n"
    "s_and_b32 s60, s12, 15\n"
    "s_add_u32 s60, s60, s47\n"
    "s_sub_u32 s58, s60, 10\n"
    "s_cmp_ge_u32 s60, 10\n"
    "s_cselect_b32 s60, s58, s60\n"
    "s_lshl_b32 s58, s60, 7\n"
    "s_mul_i32 s59, s60, 196608\n"
    "s_mul_i32 s61, s60, s53\n"
    "s_add_u32 m0, s96, 41088\n"
    "s_nop 0\n"
    "buffer_load_dwordx4 v2, s[16:19], s58 offen offset:0 lds\n"
    "buffer_load_dwordx4 v3, s[16:19], s58 offen offset:1024 lds\n"
    "buffer_load_dwordx4 v4, s[16:19], s58 offen offset:2048 lds\n"
    "buffer_load_dwordx4 v5, s[16:19], s58 offen offset:3072 lds\n"
    "s_add_u32 m0, s51, 41088\n"
    "s_nop 0\n"
    "buffer_load_dwordx4 v8, s[32:35], s61 offen offset:0 lds\n"
    "s_add_u32 m0, s52, 41088\n"
    "s_nop 0\n"
    "buffer_load_dwordx4 v9, s[36:39], s61 offen offset:0 lds\n"
    "s_add_u32 m0, s97, 41088\n"
    "s_nop 0\n"
    "buffer_load_dwordx4 v6, s[20:23], s59 offen offset:0 lds\n"
    "buffer_load_dwordx4 v7, s[20:23], s59 offen offset:1024 lds\n"
    "s_add_u32 m0, s50, 41088\n"
    "s_nop 0\n"
    "buffer_load_dwordx4 v6, s[24:27], s59 offen offset:0 lds\n"
    "buffer_load_dwordx4 v7, s[24:27], s59 offen offset:1024 lds\n"
    "s_waitcnt vmcnt(10)\n"
    "s_barrier\n"
    "s_cmp_lg_u32 s44, 0\n"
    "s_cbranch_scc1 Lpk6_%=\n"
    "s_cmp_ge_u32 s46, 30\n"
    "s_cbranch_scc1 Lpk6_%=\n"
    "v_mov_b32 v16, s62\n"
    "v_mov_b32 v17, s63\n"
    "v_lshlrev_b32 v15, 3, v0\n"
    "s_mul_i32 s9, s2, 32\n"
    "s_add_u32 s9, s9, 4096\n"
    "s_mov_b64 exec, 15\n"
    "buffer_store_dwordx2 v[16:17], v15, s[40:43], s9 offen sc1\n"
    "s_mov_b64 exec, -1\n"
    "Lpk6_%=:\n"
    "s_lshr_b64 s[12:13], s[12:13], 4\n"
    "s_and_b32 s60, s12, 15\n"
    "s_add_u32 s60, s60, s47\n"
    "s_sub_u32 s58, s60, 10\n"
    "s_cmp_ge_u32 s60, 10\n"
    "s_cselect_b32 s60, s58, s60\n"
    "s_lshl_b32 s58, s60, 7\n"
    "s_mul_i32 s59, s60, 196608\n"
    "s_mul_i32 s61, s60, s53\n"
    "s_add_u32 m0, s96, 82176\n"
    "s_nop 0\n"
    "buffer_load_dwordx4 v2, s[16:19], s58 offen offset:0 lds\n"
    "buffer_load_dwordx4 v3, s[16:19], s58 offen offset:1024 lds\n"
    "buffer_load_dwordx4 v4, s[16:19], s58 offen offset:2048 lds\n"
    "buffer_load_dwordx4 v5, s[16:19], s58 offen offset:3072 lds\n"
    "s_add_u32 m0, s51, 82176\n"
    "s_nop 0\n"
    "buffer_load_dwordx4 v8, s[32:35], s61 offen offset:0 lds\n"
    "s_add_u32 m0, s52, 82176\n"
    "s_nop 0\n"
    "buffer_load_dwordx4 v9, s[36:39], s61 offen offset:0 lds\n"
    "s_add_u32 m0, s97, 82176\n"
    "s_nop 0\n"
    "buffer_load_dwordx4 v6, s[20:23], s59 offen offset:0 lds\n"
    "buffer_load_dwordx4 v7, s[20:23], s59 offen offset:1024 lds\n"
    "s_add_u32 m0, s50, 82176\n"
    "s_nop 0\n"
    "buffer_load_dwordx4 v6, s[24:27], s59 offen offset:0 lds\n"
    "buffer_load_dwordx4 v7, s[24:27], s59 offen offset:1024 lds\n"
    "s_waitcnt vmcnt(10)\n"
    "s_barrier\n"
    "s_lshr_b64 s[12:13], s[12:13], 4\n"
    "s_and_b32 s60, s12, 15\n"
    "s_add_u32 s60, s60, s47\n"
    "s_sub_u32 s58, s60, 10\n"
    "s_cmp_ge_u32 s60, 10\n"
    "s_cselect_b32 s60, s58, s60\n"
    "s_lshl_b32 s58, s60, 7\n"
    "s_mul_i32 s59, s60, 196608\n"
    "s_mul_i32 s61, s60, s53\n"
    "s_add_u32 m0, s96, 0\n"
    "s_nop 0\n"
    "buffer_load_dwordx4 v2, s[16:19], s58 offen offset:0 lds\n"
    "buffer_load_dwordx4 v3, s[16:19], s58 offen offset:1024 lds\n"
    "buffer_load_dwordx4 v4, s[16:19], s58 offen offset:2048 lds\n"
    "buffer_load_dwordx4 v5, s[16:19], s58 offen offset:3072 lds\n"
    "s_add_u32 m0, s51, 0\n"
    "s_nop 0\n"
    "buffer_load_dwordx4 v8, s[32:35], s61 offen offset:0 lds\n"
    "s_add_u32 m0, s52, 0\n"
    "s_nop 0\n"
    "buffer_load_dwordx4 v9, s[36:39], s61 offen offset:0 lds\n"
    "s_add_u32 m0, s97, 0\n"
    "s_nop 0\n"
    "buffer_load_dwordx4 v6, s[20:23], s59 offen offset:0 lds\n"
    "buffer_load_dwordx4 v7, s[20:23], s59 offen offset:1024 lds\n"
    "s_add_u32 m0, s50, 0\n"
    "s_nop 0\n"
    "buffer_load_dwordx4 v6, s[24:27], s59 offen offset:0 lds\n"
    "buffer_load_dwordx4 v7, s[24:27], s59 offen offset:1024 lds\n"
    "s_waitcnt vmcnt(10)\n"
    "s_barrier\n"
    "s_lshr_b64 s[12:13], s[12:13], 4\n"
    "s_and_b32 s60, s12, 15\n"
    "s_add_u32 s60, s60, s47\n"
    "s_sub_u32 s58, s60, 10\n"
    "s_cmp_ge_u32 s60, 10\n"
    "s_cselect_b32 s60, s58, s60\n"
    "s_lshl_b32 s58, s60, 7\n"
    "s_mul_i32 s59, s60, 196608\n"
    "s_mul_i32 s61, s60, s53\n"
    "s_add_u32 m0, s96, 41088\n"
    "s_nop 0\n"
    "buffer_load_dwordx4 v2, s[16:19], s58 offen offset:0 lds\n"
    "buffer_load_dwordx4 v3, s[16:19], s58 offen offset:1024 lds\n"
    "buffer_load_dwordx4 v4, s[16:19], s58 offen offset:2048 lds\n"
    "buffer_load_dwordx4 v5, s[16:19], s58 offen offset:3072 lds\n"
    "s_add_u32 m0, s51, 41088\n"
    "s_nop 0\n"
    "buffer_load_dwordx4 v8, s[32:35], s61 offen offset:0 lds\n"
    "s_add_u32 m0, s52, 41088\n"
    "s_nop 0\n"
    "buffer_load_dwordx4 v9, s[36:39], s61 offen offset:0 lds\n"
    "s_add_u32 m0, s97, 41088\n"
    "s_nop 0\n"
    "buffer_load_dwordx4 v6, s[20:23], s59 offen offset:0 lds\n"
    "buffer_load_dwordx4 v7, s[20:23], s59 offen offset:1024 lds\n"
    "s_add_u32 m0, s50, 41088\n"
    "s_nop 0\n"
    "buffer_load_dwordx4 v6, s[24:27], s59 offen offset:0 lds\n"
    "buffer_load_dwordx4 v7, s[24:27], s59 offen offset:1024 lds\n"
    "s_and_b32 s55, s46, 7\n"
    "s_lshr_b32 s54, s45, 1\n"
    "s_lshl3_add_u32 s10, s54, s55\n"
    "s_lshr_b32 s54, s46, 3\n"
    "s_and_b32 s11, s45, 1\n"
    "s_lshl2_add_u32 s11, s11, s54\n"
    "s_and_b32 s28, s46, 1\n"
    "s_mul_i32 s28, s28, 6\n"
    "s_and_b32 s8, s44, 1\n"
    "s_mul_i32 s8, s8, 24\n"
    "s_lshr_b32 s9, s44, 1\n"
    "s_add_u32 s8, s8, s9\n"
    "s_mul_i32 s9, s11, 48\n"
    "s_add_u32 s8, s8, s9\n"
    "s_mul_i32 s8, s8, 5120\n"
    "s_mul_i32 s9, s28, 10240\n"
    "s_add_u32 s29, s8, s9\n"
    "s_sub_u32 s28, s8, s9\n"
    "s_add_u32 s28, s28, 61440\n"
    "s_mul_i32 s30, s44, 5120\n"
    "v_and_b32 v14, 1, v0\n"
    "v_lshlrev_b32 v14, 3, v14\n"
    "s_and_b32 s8, s44, 1\n"
    "s_lshl2_add_u32 s8, s8, 143744\n"
    "v_add_u32 v14, s8, v14\n"
    "s_add_u32 s8, s30, 82176\n"
    "v_add_u32 v24, s8, v23\n"
    "s_waitcnt vmcnt(10)\n"
    "s_barrier\n"
    "s_lshr_b64 s[12:13], s[12:13], 4\n"
    "s_and_b32 s60, s12, 15\n"
    "s_add_u32 s60, s60, s47\n"
    "s_sub_u32 s58, s60, 10\n"
    "s_cmp_ge_u32 s60, 10\n"
    "s_cselect_b32 s60, s58, s60\n"
    "s_lshl_b32 s58, s60, 7\n"
    "s_mul_i32 s59, s60, 196608\n"
    "s_mul_i32 s61, s60, s53\n"
    "s_add_u32 m0, s96, 82176\n"
    "s_nop 0\n"
    "buffer_load_dwordx4 v2, s[16:19], s58 offen offset:0 lds\n"
    "buffer_load_dwordx4 v3, s[16:19], s58 offen offset:1024 lds\n"
    "buffer_load_dwordx4 v4, s[16:19], s58 offen offset:2048 lds\n"
    "buffer_load_dwordx4 v5, s[16:19], s58 offen offset:3072 lds\n"
    "s_add_u32 m0, s51, 82176\n"
    "s_nop 0\n"
    "buffer_load_dwordx4 v8, s[32:35], s61 offen offset:0 lds\n"
    "s_add_u32 m0, s52, 82176\n"
    "s_nop 0\n"
    "buffer_load_dwordx4 v9, s[36:39], s61 offen offset:0 lds\n"
    "s_add_u32 m0, s97, 82176\n"
    "s_nop 0\n"
    "buffer_load_dwordx4 v6, s[20:23], s59 offen offset:0 lds\n"
    "buffer_load_dwordx4 v7, s[20:23], s59 offen offset:1024 lds\n"
    "s_add_u32 m0, s50, 82176\n"
    "s_nop 0\n"
    "buffer_load_dwordx4 v6, s[24:27], s59 offen offset:0 lds\n"
    "buffer_load_dwordx4 v7, s[24:27], s59 offen offset:1024 lds\n"
    "s_branch LL2_%=\n"
    "LC_%=:\n"
    "v_lshlrev_b32 v8, 2, v0\n"
    "v_mov_b32 v18, 0\n"
    "v_add_u32 v8, 143744, v8\n"
    "ds_write_b32 v8, v18\n"
    "s_setprio 2\n"
    "s_sub_u32 s96, s44, 4\n"
    "s_lshr_b32 s57, s45, 1\n"
    "s_mul_i32 s48, s57, 6\n"
    "s_add_u32 s48, s48, s49\n"
    "s_lshl1_add_u32 s55, s45, s46\n"
    "s_sub_u32 s55, s55, 30\n"
    "s_cmp_ge_u32 s46, 30\n"
    "s_cselect_b32 s48, s55, s48\n"
    "s_lshr_b32 s53, s47, 3\n"
    "s_add_u32 s53, s53, s48\n"
    "s_mul_i32 s54, s45, 30\n"
    "s_add_u32 s54, s54, s46\n"
    "s_mul_i32 s56, s54, 205\n"
    "s_lshr_b32 s56, s56, 10\n"
    "s_mul_i32 s55, s56, 5\n"
    "s_sub_u32 s57, s54, s55\n"
    "s_lshl2_add_u32 s57, s57, s96\n"
    "s_mul_i32 s54, s56, 5\n"
    "s_lshl1_add_u32 s60, s57, 0\n"
    "s_mul_i32 s55, s60, 52\n"
    "s_lshr_b32 s55, s55, 8\n"
    "s_mul_i32 s55, s55, 235\n"
    "s_add_u32 s60, s60, s55\n"
    "s_add_u32 s60, s60, s54\n"
    "s_lshl_b32 s56, s60, 10\n"
    "s_lshl1_add_u32 s60, s57, 1\n"
    "s_mul_i32 s55, s60, 52\n"
    "s_lshr_b32 s55, s55, 8\n"
    "s_mul_i32 s55, s55, 235\n"
    "s_add_u32 s60, s60, s55\n"
    "s_add_u32 s60, s60, s54\n"
    "s_lshl_b32 s57, s60, 10\n"
    "v_lshrrev_b32 v14, 2, v0\n"
    "v_and_b32 v13, 3, v0\n"
    "s_lshl_b32 s60, s96, 6\n"
    "s_add_u32 s60, s60, 144256\n"
    "v_lshlrev_b32 v14, 8, v14\n"
    "v_lshl_add_u32 v14, v13, 3, v14\n"
    "v_add_u32 v14, s60, v14\n"
    "s_waitcnt lgkmcnt(0)\n"
    "s_add_u32 s4, s14, 8388608\n"
    "s_addc_u32 s5, s15, 0\n"
    "s_and_b32 s5, s5, 0xffff\n"
    "s_mov_b32 s6, 1966080\n"
    "s_mov_b32 s7, 0x20000\n"
    "s_mov_b32 s34, 6291456\n"
    "s_mov_b32 s35, 0x20000\n"
    "s_mov_b32 s38, 2621440\n"
    "s_mov_b32 s39, 0x20000\n"
    "s_mov_b32 s42, 1048576\n"
    "s_mov_b32 s43, 0x20000\n"
    "s_add_u32 s32, s14, 1048576\n"
    "s_addc_u32 s33, s15, 0\n"
    "s_and_b32 s33, s33, 0xffff\n"
    "s_mov_b32 s36, s12\n"
    "s_and_b32 s37, s13, 0xffff\n"
    "s_mov_b32 s40, s14\n"
    "s_and_b32 s41, s15, 0xffff\n"
    "s_lshl2_add_u32 s8, s96, 143744\n"
    "v_mov_b32 v17, s8\n"
    "s_and_b32 s55, s46, 7\n"
    "s_lshr_b32 s54, s45, 1\n"
    "s_lshl3_add_u32 s50, s54, s55\n"
    "s_lshr_b32 s54, s46, 3\n"
    "s_and_b32 s51, s45, 1\n"
    "s_lshl2_add_u32 s51, s51, s54\n"
    "s_and_b32 s52, s46, 1\n"
    "s_mul_i32 s52, s52, 6\n"
    "s_and_b32 s58, s96, 1\n"
    "s_lshr_b32 s59, s96, 1\n"
    "s_lshl1_add_u32 s8, s50, s59\n"
    "s_mul_i32 s8, s8, 98304\n"
    "s_mul_i32 s9, s58, 73728\n"
    "s_add_u32 s9, s8, s9\n"
    "s_mul_i32 s10, s58, -24576\n"
    "s_add_u32 s8, s8, s10\n"
    "s_add_u32 s8, s8, 49152\n"
    "v_lshlrev_b32 v8, 4, v0\n"
    "v_add_u32 v144, s8, v8\n"
    "v_add_u32 v145, s9, v8\n"
    "s_mul_i32 s8, s58, 5120\n"
    "s_add_u32 s9, s8, 123264\n"
    "v_add_u32 v146, s9, v8\n"
    "s_add_u32 s9, s8, 41088\n"
    "v_add_u32 v147, s9, v8\n"
    "s_add_u32 s9, s8, 82176\n"
    "v_add_u32 v148, s9, v8\n"
    "s_add_u32 s9, s8, 0\n"
    "v_add_u32 v149, s9, v8\n"
    "s_lshl1_add_u32 s8, s50, s59\n"
    "s_mul_i32 s9, s8, 43\n"
    "s_lshr_b32 s9, s9, 7\n"
    "s_and_b32 s10, s9, 1\n"
    "s_lshr_b32 s9, s9, 1\n"
    "s_and_b32 s11, s50, 1\n"
    "s_cmp_ge_u32 s50, 30\n"
    "s_cselect_b32 s10, s11, s10\n"
    "s_cselect_b32 s8, 16, 24\n"
    "s_mul_i32 s9, s9, 48\n"
    "s_mul_i32 s11, s8, s58\n"
    "s_add_u32 s9, s9, s11\n"
    "s_sub_u32 s8, s8, 1\n"
    "v_min_u32 v150, s8, v0\n"
    "v_add_u32 v150, s9, v150\n"
    "v_lshlrev_b32 v150, 8, v150\n"
    "s_lshl_b32 s10, s10, 7\n"
    "s_add_u32 s10, s10, 393216\n"
    "v_add_u32 v150, s10, v150\n"
    "s_lshl2_add_u32 s8, s53, s96\n"
    "s_lshl_b32 s8, s8, 7\n"
    "s_add_u32 s8, s8, 393216\n"
    "v_lshlrev_b32 v157, 3, v0\n"
    "v_add_u32 v157, s8, v157\n"
    "v_and_b32 v151, 1, v0\n"
    "v_lshlrev_b32 v151, 3, v151\n"
    "s_lshl2_add_u32 s8, s58, 143744\n"
    "v_add_u32 v151, s8, v151\n"
    "s_lshl1_add_u32 s8, s50, s59\n"
    "s_lshl1_add_u32 s8, s8, s58\n"
    "s_mul_i32 s8, s8, 20480\n"
    "s_mul_i32 s9, s51, 160\n"
    "s_add_u32 s8, s8, s9\n"
    "v_and_b32 v152, 15, v0\n"
    "v_mul_u32_u24 v152, 1280, v152\n"
    "v_add_u32 v152, s8, v152\n"
    "v_lshrrev_b32 v9, 4, v0\n"
    "v_lshl_add_u32 v153, v9, 3, v152\n"
    "v_and_b32 v9, 1, v9\n"
    "v_lshl_add_u32 v152, v9, 5, v152\n"
    "v_lshrrev_b32 v9, 5, v0\n"
    "v_lshl_add_u32 v152, v9, 4, v152\n"
    "s_mul_i32 s8, s96, 5120\n"
    "s_add_u32 s8, s8, 20480\n"
    "v_add_u32 v154, s8, v8\n"
    "s_xor_b32 s9, s96, 1\n"
    "s_mul_i32 s8, s9, 5120\n"
    "s_add_u32 s8, s8, 20480\n"
    "v_add_u32 v155, s8, v8\n"
    "s_lshl2_add_u32 s8, s9, 143744\n"
    "v_mov_b32 v156, s8\n"
    "v_mov_b32 v158, 144000\n"
    "v_mov_b32 v160, 0\n"
    "v_mov_b32 v161, 0\n"
    "v_mov_b32 v162, 0\n"
    "v_mov_b32 v163, 0\n"
    "v_mov_b32 v164, 0\n"
    "v_mov_b32 v165, 0\n"
    "v_mov_b32 v166, 0\n"
    "v_mov_b32 v167, 0\n"
    "v_mov_b32 v168, 0\n"
    "v_mov_b32 v169, 0\n"
    "v_mov_b32 v170, 0\n"
    "v_mov_b32 v171, 0\n"
    "v_mov_b32 v172, 0\n"
    "v_mov_b32 v173, 0\n"
    "v_mov_b32 v174, 0\n"
    "v_mov_b32 v175, 0\n"
    "v_mov_b32 v176, 0\n"
    "v_mov_b32 v177, 0\n"
    "v_mov_b32 v178, 0\n"
    "v_mov_b32 v179, 0\n"
    "v_mov_b32 v180, 0\n"
    "v_mov_b32 v181, 0\n"
    "v_mov_b32 v182, 0\n"
    "v_mov_b32 v183, 0\n"
    "v_mov_b32 v184, 0\n"
    "v_mov_b32 v185, 0\n"
    "v_mov_b32 v186, 0\n"
    "v_mov_b32 v187, 0\n"
    "v_mov_b32 v188, 0\n"
    "v_mov_b32 v189, 0\n"
    "v_mov_b32 v190, 0\n"
    "v_mov_b32 v191, 0\n"
    "v_mov_b32 v192, 0\n"
    "v_mov_b32 v193, 0\n"
    "v_mov_b32 v194, 0\n"
    "v_mov_b32 v195, 0\n"
    "v_mov_b32 v196, 0\n"
    "v_mov_b32 v197, 0\n"
    "v_mov_b32 v198, 0\n"
    "v_mov_b32 v199, 0\n"
    "v_mov_b32 v200, 0\n"
    "v_mov_b32 v201, 0\n"
    "v_mov_b32 v202, 0\n"
    "v_mov_b32 v203, 0\n"
    "v_mov_b32 v204, 0\n"
    "v_mov_b32 v205, 0\n"
    "v_mov_b32 v206, 0\n"
    "v_mov_b32 v207, 0\n"
    "v_mov_b32 v208, 0\n"
    "v_mov_b32 v209, 0\n"
    "v_mov_b32 v210, 0\n"
    "v_mov_b32 v211, 0\n"
    "v_mov_b32 v212, 0\n"
    "v_mov_b32 v213, 0\n"
    "v_mov_b32 v214, 0\n"
    "v_mov_b32 v215, 0\n"
    "v_mov_b32 v216, 0\n"
    "v_mov_b32 v217, 0\n"
    "v_mov_b32 v218, 0\n"
    "v_mov_b32 v219, 0\n"
    "v_mov_b32 v220, 0\n"
    "v_mov_b32 v221, 0\n"
    "v_mov_b32 v222, 0\n"
    "v_mov_b32 v223, 0\n"
    "v_mov_b32 v224, 0\n"
    "v_mov_b32 v225, 0\n"
    "v_mov_b32 v226, 0\n"
    "v_mov_b32 v227, 0\n"
    "v_mov_b32 v228, 0\n"
    "v_mov_b32 v229, 0\n"
    "v_mov_b32 v230, 0\n"
    "v_mov_b32 v231, 0\n"
    "v_mov_b32 v232, 0\n"
    "v_mov_b32 v233, 0\n"
    "v_mov_b32 v234, 0\n"
    "v_mov_b32 v235, 0\n"
    "v_mov_b32 v236, 0\n"
    "v_mov_b32 v237, 0\n"
    "v_mov_b32 v238, 0\n"
    "v_mov_b32 v239, 0\n"
    "v_mov_b32 v240, 0\n"
    "v_mov_b32 v241, 0\n"
    "v_mov_b32 v242, 0\n"
    "v_mov_b32 v243, 0\n"
    "v_mov_b32 v244, 0\n"
    "v_mov_b32 v245, 0\n"
    "v_mov_b32 v246, 0\n"
    "v_mov_b32 v247, 0\n"
    "v_mov_b32 v248, 0\n"
    "v_mov_b32 v249, 0\n"
    "v_mov_b32 v250, 0\n"
    "v_mov_b32 v251, 0\n"
    "v_mov_b32 v252, 0\n"
    "v_mov_b32 v253, 0\n"
    "v_mov_b32 v254, 0\n"
    "v_mov_b32 v255, 0\n"
    "s_mov_b32 s97, 0\n"
    "s_mov_b32 s12, 41088\n"
    "s_mov_b32 s54, 0xbfb8aa3b\n"
    "s_mov_b32 s55, 0xbfb8aa3b\n"
    "s_cmp_ge_u32 s46, 30\n"
    "s_cbranch_scc1 LCodd_%=\n"
    "v_and_b32 v8, 15, v0\n"
    "v_lshrrev_b32 v9, 4, v0\n"
    "v_bfe_u32 v10, v0, 1, 3\n"
    "v_xor_b32 v10, v10, v9\n"
    "v_lshlrev_b32 v10, 4, v10\n"
    "v_lshl_add_u32 v10, v8, 7, v10\n"
    "s_and_b32 s8, s96, 1\n"
    "s_mul_i32 s9, s8, 12288\n"
    "v_add_u32 v2, s9, v10\n"
    "v_xor_b32 v3, 64, v2\n"
    "v_bfe_u32 v10, v0, 2, 2\n"
    "v_lshl_add_u32 v10, v9, 3, v10\n"
    "v_lshlrev_b32 v10, 7, v10\n"
    "v_bfe_u32 v11, v0, 3, 2\n"
    "s_and_b32 s9, s96, 2\n"
    "v_xor_b32 v11, s9, v11\n"
    "v_lshl_add_u32 v10, v11, 5, v10\n"
    "v_and_b32 v11, 3, v0\n"
    "v_lshl_add_u32 v10, v11, 3, v10\n"
    "v_add_u32 v4, 24576, v10\n"
    "v_xor_b32 v5, 32, v4\n"
    "s_lshr_b32 s9, s47, 4\n"
    "s_mul_i32 s8, s8, 6\n"
    "s_add_u32 s8, s8, s9\n"
    "s_mul_i32 s8, s8, 48\n"
    "s_lshr_b32 s9, s96, 1\n"
    "s_lshl1_add_u32 s9, s48, s9\n"
    "s_add_u32 s8, s8, s9\n"
    "s_lshl_b32 s60, s8, 10\n"
    "v_lshlrev_b32 v20, 4, v0\n"
    "s_mov_b32 s61, 4\n"
    "s_mov_b32 s59, 49152\n"
    "s_mov_b32 s13, 49152\n"
    "s_branch LCst_%=\n"
    "LCodd_%=:\n"
    "v_and_b32 v8, 31, v0\n"
    "v_lshrrev_b32 v9, 5, v0\n"
    "v_bfe_u32 v10, v0, 1, 3\n"
    "v_xor_b32 v10, v10, v9\n"
    "v_lshlrev_b32 v10, 4, v10\n"
    "v_lshl_add_u32 v10, v8, 7, v10\n"
    "s_and_b32 s8, s96, 1\n"
    "s_lshl_b32 s9, s8, 13\n"
    "v_add_u32 v2, s9, v10\n"
    "v_xor_b32 v3, 32, v2\n"
    "v_xor_b32 v4, 64, v2\n"
    "v_xor_b32 v5, 96, v2\n"
    "v_bfe_u32 v10, v0, 4, 1\n"
    "v_mul_u32_u24 v10, 12304, v10\n"
    "v_bfe_u32 v11, v0, 2, 2\n"
    "v_lshl_add_u32 v11, v9, 3, v11\n"
    "v_mul_u32_u24 v11, 192, v11\n"
    "v_and_b32 v12, 1, v0\n"
    "v_lshl_add_u32 v10, v12, 3, v10\n"
    "v_bfe_u32 v12, v0, 1, 1\n"
    "v_lshl_add_u32 v10, v12, 5, v10\n"
    "s_lshr_b32 s10, s96, 1\n"
    "s_lshl_b32 s9, s10, 4\n"
    "s_add_u32 s9, s9, 16384\n"
    "v_add3_u32 v6, v10, v11, s9\n"
    "v_lshrrev_b32 v10, 4, v8\n"
    "v_mul_u32_u24 v10, 49152, v10\n"
    "v_and_b32 v11, 15, v0\n"
    "v_lshlrev_b32 v11, 4, v11\n"
    "v_lshl_add_u32 v11, v9, 8, v11\n"
    "s_lshl2_add_u32 s8, s8, 120\n"
    "s_mul_i32 s8, s8, 49152\n"
    "s_mul_i32 s9, s48, 3072\n"
    "s_add_u32 s8, s8, s9\n"
    "s_lshl_b32 s10, s10, 9\n"
    "s_add_u32 s8, s8, s10\n"
    "v_add3_u32 v20, v10, v11, s8\n"
    "s_mov_b32 s61, 10\n"
    "s_mov_b32 s60, 0\n"
    "s_mov_b32 s59, 1024\n"
    "s_mov_b32 s13, 96256\n"
    "s_barrier\n"
    "ds_read_b128 v[56:59], v2\n"
    "ds_read_b64_tr_b16 v[64:65], v6 offset:0\n"
    "ds_read_b64_tr_b16 v[66:67], v6 offset:768\n"
    "ds_read_b64_tr_b16 v[68:69], v6 offset:64\n"
    "ds_read_b64_tr_b16 v[70:71], v6 offset:832\n"
    "ds_read_b64_tr_b16 v[72:73], v6 offset:128\n"
    "ds_read_b64_tr_b16 v[74:75], v6 offset:896\n"
    "ds_read_b128 v[60:63], v2 offset:4096\n"
    "LCo_%=:\n"
    "s_waitcnt lgkmcnt(5)\n"
    "v_mfma_f32_32x32x16_bf16 v[160:175], v[64:67], v[56:59], v[160:175]\n"
    "ds_read_b128 v[76:79], v3\n"
    "ds_read_b64_tr_b16 v[84:85], v6 offset:3072\n"
    "s_waitcnt lgkmcnt(5)\n"
    "v_mfma_f32_32x32x16_bf16 v[176:191], v[68:71], v[56:59], v[176:191]\n"
    "ds_read_b64_tr_b16 v[86:87], v6 offset:3840\n"
    "ds_read_b64_tr_b16 v[88:89], v6 offset:3136\n"
    "s_waitcnt lgkmcnt(5)\n"
    "v_mfma_f32_32x32x16_bf16 v[192:207], v[72:75], v[56:59], v[192:207]\n"
    "ds_read_b64_tr_b16 v[90:91], v6 offset:3904\n"
    "ds_read_b64_tr_b16 v[92:93], v6 offset:3200\n"
    "s_waitcnt lgkmcnt(6)\n"
    "v_mfma_f32_32x32x16_bf16 v[208:223], v[64:67], v[60:63], v[208:223]\n"
    "ds_read_b64_tr_b16 v[94:95], v6 offset:3968\n"
    "v_mfma_f32_32x32x16_bf16 v[224:239], v[68:71], v[60:63], v[224:239]\n"
    "ds_read_b128 v[80:83], v3 offset:4096\n"
    "s_add_u32 s97, s97, 1\n"
    "s_cmp_eq_u32 s97, 3\n"
    "v_mfma_f32_32x32x16_bf16 v[240:255], v[72:75], v[60:63], v[240:255]\n"
    "s_cselect_b32 s97, 0, s97\n"
    "s_cselect_b32 s58, 4294885120, s12\n"
    "s_waitcnt lgkmcnt(5)\n"
    "v_mfma_f32_32x32x16_bf16 v[160:175], v[84:87], v[76:79], v[160:175]\n"
    "ds_read_b128 v[96:99], v4\n"
    "ds_read_b64_tr_b16 v[104:105], v6 offset:6144\n"
    "s_waitcnt lgkmcnt(5)\n"
    "v_mfma_f32_32x32x16_bf16 v[176:191], v[88:91], v[76:79], v[176:191]\n"
    "ds_read_b64_tr_b16 v[106:107], v6 offset:6912\n"
    "ds_read_b64_tr_b16 v[108:109], v6 offset:6208\n"
    "s_waitcnt lgkmcnt(5)\n"
    "v_mfma_f32_32x32x16_bf16 v[192:207], v[92:95], v[76:79], v[192:207]\n"
    "ds_read_b64_tr_b16 v[110:111], v6 offset:6976\n"
    "ds_read_b64_tr_b16 v[112:113], v6 offset:6272\n"
    "s_waitcnt lgkmcnt(6)\n"
    "v_mfma_f32_32x32x16_bf16 v[208:223], v[84:87], v[80:83], v[208:223]\n"
    "ds_read_b64_tr_b16 v[114:115], v6 offset:7040\n"
    "v_add_u32 v2, s58, v2\n"
    "v_add_u32 v3, s58, v3\n"
    "v_mfma_f32_32x32x16_bf16 v[224:239], v[88:91], v[80:83], v[224:239]\n"
    "ds_read_b128 v[100:103], v4 offset:4096\n"
    "v_mfma_f32_32x32x16_bf16 v[240:255], v[92:95], v[80:83], v[240:255]\n"
    "v_add_u32 v4, s58, v4\n"
    "s_waitcnt lgkmcnt(5)\n"
    "v_mfma_f32_32x32x16_bf16 v[160:175], v[104:107], v[96:99], v[160:175]\n"
    "ds_read_b128 v[116:119], v5\n"
    "ds_read_b64_tr_b16 v[124:125], v6 offset:9216\n"
    "s_waitcnt lgkmcnt(5)\n"
    "v_mfma_f32_32x32x16_bf16 v[176:191], v[108:111], v[96:99], v[176:191]\n"
    "ds_read_b64_tr_b16 v[126:127], v6 offset:9984\n"
    "ds_read_b64_tr_b16 v[128:129], v6 offset:9280\n"
    "s_waitcnt lgkmcnt(5)\n"
    "v_mfma_f32_32x32x16_bf16 v[192:207], v[112:115], v[96:99], v[192:207]\n"
    "ds_read_b64_tr_b16 v[130:131], v6 offset:10048\n"
    "ds_read_b64_tr_b16 v[132:133], v6 offset:9344\n"
    "s_waitcnt lgkmcnt(6)\n"
    "v_mfma_f32_32x32x16_bf16 v[208:223], v[104:107], v[100:103], v[208:223]\n"
    "ds_read_b64_tr_b16 v[134:135], v6 offset:10112\n"
    "v_mfma_f32_32x32x16_bf16 v[224:239], v[108:111], v[100:103], v[224:239]\n"
    "ds_read_b128 v[120:123], v5 offset:4096\n"
    "v_add_u32 v5, s58, v5\n"
    "v_add_u32 v6, s58, v6\n"
    "v_mfma_f32_32x32x16_bf16 v[240:255], v[112:115], v[100:103], v[240:255]\n"
    "s_cmp_eq_u32 s61, 1\n"
    "s_waitcnt lgkmcnt(5)\n"
    "v_mfma_f32_32x32x16_bf16 v[160:175], v[124:127], v[116:119], v[160:175]\n"
    "s_waitcnt lgkmcnt(0)\n"
    "s_cbranch_scc1 Lob17_%=\n"
    "s_barrier\n"
    "Lob17_%=:\n"
    "ds_read_b128 v[56:59], v2\n"
    "ds_read_b64_tr_b16 v[64:65], v6 offset:0\n"
    "v_mfma_f32_32x32x16_bf16 v[176:191], v[128:131], v[116:119], v[176:191]\n"
    "ds_read_b64_tr_b16 v[66:67], v6 offset:768\n"
    "ds_read_b64_tr_b16 v[68:69], v6 offset:64\n"
    "v_mfma_f32_32x32x16_bf16 v[192:207], v[132:135], v[116:119], v[192:207]\n"
    "ds_read_b64_tr_b16 v[70:71], v6 offset:832\n"
    "ds_read_b64_tr_b16 v[72:73], v6 offset:128\n"
    "v_mfma_f32_32x32x16_bf16 v[208:223], v[124:127], v[120:123], v[208:223]\n"
    "ds_read_b64_tr_b16 v[74:75], v6 offset:896\n"
    "v_mfma_f32_32x32x16_bf16 v[224:239], v[128:131], v[120:123], v[224:239]\n"
    "ds_read_b128 v[60:63], v2 offset:4096\n"
    "s_sub_u32 s61, s61, 1\n"
    "s_cmp_lg_u32 s61, 0\n"
    "v_mfma_f32_32x32x16_bf16 v[240:255], v[132:135], v[120:123], v[240:255]\n"
    "s_cbranch_scc1 LCo_%=\n"
    "ds_read_b64 v[158:159], v158\n"
    "s_waitcnt lgkmcnt(0)\n"
    "v_add_co_u32 v158, vcc, 1, v158\n"
    "s_nop 1\n"
    "v_addc_co_u32 v159, vcc, 0, v159, vcc\n"
    "s_nop 1\n"
    "v_pk_mul_f32 v[24:25], v[160:161], s[54:55]\n"
    "v_pk_mul_f32 v[26:27], v[162:163], s[54:55]\n"
    "v_pk_mul_f32 v[28:29], v[164:165], s[54:55]\n"
    "v_pk_mul_f32 v[30:31], v[166:167], s[54:55]\n"
    "v_exp_f32 v24, v24\n"
    "v_exp_f32 v25, v25\n"
    "v_exp_f32 v26, v26\n"
    "v_exp_f32 v27, v27\n"
    "v_exp_f32 v28, v28\n"
    "v_exp_f32 v29, v29\n"
    "v_exp_f32 v30, v30\n"
    "v_exp_f32 v31, v31\n"
    "v_pk_add_f32 v[24:25], v[24:25], 1.0 op_sel_hi:[1,0]\n"
    "v_pk_add_f32 v[26:27], v[26:27], 1.0 op_sel_hi:[1,0]\n"
    "v_pk_add_f32 v[28:29], v[28:29], 1.0 op_sel_hi:[1,0]\n"
    "v_pk_add_f32 v[30:31], v[30:31], 1.0 op_sel_hi:[1,0]\n"
    "v_rcp_f32 v24, v24\n"
    "v_rcp_f32 v25, v25\n"
    "v_rcp_f32 v26, v26\n"
    "v_rcp_f32 v27, v27\n"
    "v_rcp_f32 v28, v28\n"
    "v_rcp_f32 v29, v29\n"
    "v_rcp_f32 v30, v30\n"
    "v_rcp_f32 v31, v31\n"
    "v_pk_mul_f32 v[24:25], v[24:25], v[168:169]\n"
    "v_pk_mul_f32 v[26:27], v[26:27], v[170:171]\n"
    "v_pk_mul_f32 v[28:29], v[28:29], v[172:173]\n"
    "v_pk_mul_f32 v[30:31], v[30:31], v[174:175]\n"
    "v_cvt_pk_bf16_f32 v160, v24, v25\n"
    "v_cvt_pk_bf16_f32 v161, v26, v27\n"
    "v_cvt_pk_bf16_f32 v162, v28, v29\n"
    "v_cvt_pk_bf16_f32 v163, v30, v31\n"
    "buffer_store_dwordx4 v[160:163], v20, s[32:35], s60 offen offset:0 sc1\n"
    "s_add_u32 s60, s60, s59\n"
    "s_branch LCepi_%=\n"
    "s_nop 0\n"
    "s_nop 0\n"
    "s_nop 0\n"
    "s_nop 0\n"
    "s_nop 0\n"
    "s_nop 0\n"
    "s_nop 0\n"
    "s_nop 0\n"
    "s_nop 0\n"
    "s_nop 0\n"
    "s_nop 0\n"
    "s_nop 0\n"
    "s_nop 0\n"
    "s_nop 0\n"
    "s_nop 0\n"
    "s_nop 0\n"
    "s_nop 0\n"
    "s_nop 0\n"
    "s_nop 0\n"
    "s_nop 0\n"
    "s_nop 0\n"
    "s_nop 0\n"
    "s_nop 0\n"
    "s_nop 0\n"
    "s_nop 0\n"
    "s_nop 0\n"
    "s_nop 0\n"
    "s_nop 0\n"
    "s_nop 0\n"
    "s_nop 0\n"
    "s_nop 0\n"
    "s_nop 0\n"
    "s_nop 0\n"
    "s_nop 0\n"
    "s_nop 0\n"
    "s_nop 0\n"
    "s_nop 0\n"
    "s_nop 0\n"
    "s_nop 0\n"
    "s_nop 0\n"
    "s_nop 0\n"
    "s_nop 0\n"
    "s_nop 0\n"
    "s_nop 0\n"
    "s_nop 0\n"
    "s_nop 0\n"
    "s_nop 0\n"
    "s_nop 0\n"
    "s_nop 0\n"
    "s_nop 0\n"
    "s_nop 0\n"
    "s_nop 0\n"
    "s_nop 0\n"
    "s_nop 0\n"
    "s_nop 0\n"
    "s_nop 0\n"
    "s_nop 0\n"
    "s_nop 0\n"
    "s_nop 0\n"
    "s_nop 0\n"
    "s_nop 0\n"
    "s_nop 0\n"
    "s_nop 0\n"
    "s_nop 0\n"
    "s_nop 0\n"
    "s_nop 0\n"
    "s_nop 0\n"
    "s_nop 0\n"
    "s_nop 0\n"
    "s_nop 0\n"
    "s_nop 0\n"
    "s_nop 0\n"
    "s_nop 0\n"
    "s_nop 0\n"
    "s_nop 0\n"
    "s_nop 0\n"
    "s_nop 0\n"
    "s_nop 0\n"
    "s_nop 0\n"
    "s_nop 0\n"
    "s_nop 0\n"
    "s_nop 0\n"
    "s_nop 0\n"
    "LPF_%=:\n"
    "ds_read_b64_tr_b16 v[24:25], v14 offset:0\n"
    "ds_read_b64_tr_b16 v[26:27], v14 offset:4096\n"
    "ds_read_b64_tr_b16 v[28:29], v14 offset:32\n"
    "ds_read_b64_tr_b16 v[30:31], v14 offset:4128\n"
    "s_waitcnt lgkmcnt(0)\n"
    "buffer_store_dwordx4 v[24:27], v20, s[4:7], s56 offen sc1\n"
    "buffer_store_dwordx4 v[28:31], v20, s[4:7], s57 offen sc1\n"
    "s_branch LPFb_%=\n"
    "LCst_%=:\n"
    "s_barrier\n"
    "ds_read_b64_tr_b16 v[56:57], v4 offset:0\n"
    "ds_read_b64_tr_b16 v[58:59], v4 offset:512\n"
    "ds_read_b64_tr_b16 v[60:61], v4 offset:4096\n"
    "ds_read_b64_tr_b16 v[62:63], v4 offset:4608\n"
    "ds_read_b128 v[120:123], v2 offset:0\n"
    "ds_read_b128 v[124:127], v3 offset:0\n"
    "ds_read_b64_tr_b16 v[64:65], v5 offset:0\n"
    "ds_read_b64_tr_b16 v[66:67], v5 offset:512\n"
    "ds_read_b64_tr_b16 v[68:69], v5 offset:4096\n"
    "ds_read_b64_tr_b16 v[70:71], v5 offset:4608\n"
    "ds_read_b64_tr_b16 v[72:73], v4 offset:8192\n"
    "ds_read_b64_tr_b16 v[74:75], v4 offset:8704\n"
    "ds_read_b64_tr_b16 v[76:77], v4 offset:12288\n"
    "ds_read_b64_tr_b16 v[78:79], v4 offset:12800\n"
    "ds_read_b64_tr_b16 v[80:81], v5 offset:8192\n"
    "ds_read_b64_tr_b16 v[82:83], v5 offset:8704\n"
    "ds_read_b64_tr_b16 v[84:85], v5 offset:12288\n"
    "ds_read_b64_tr_b16 v[86:87], v5 offset:12800\n"
    "ds_read_b128 v[128:131], v2 offset:2048\n"
    "ds_read_b128 v[132:135], v3 offset:2048\n"
    "s_waitcnt lgkmcnt(14)\n"
    "LCm_%=:\n"
    "s_waitcnt lgkmcnt(15)\n"
    "v_mfma_f32_16x16x32_bf16 v[160:163], v[56:59], v[120:123], v[160:163]\n"
    "ds_read_b128 v[136:139], v2 offset:4096\n"
    "s_waitcnt lgkmcnt(15)\n"
    "v_mfma_f32_16x16x32_bf16 v[160:163], v[60:63], v[124:127], v[160:163]\n"
    "ds_read_b128 v[140:143], v3 offset:4096\n"
    "s_waitcnt lgkmcnt(14)\n"
    "v_mfma_f32_16x16x32_bf16 v[164:167], v[64:67], v[120:123], v[164:167]\n"
    "s_waitcnt lgkmcnt(12)\n"
    "v_mfma_f32_16x16x32_bf16 v[164:167], v[68:71], v[124:127], v[164:167]\n"
    "s_waitcnt lgkmcnt(10)\n"
    "v_mfma_f32_16x16x32_bf16 v[168:171], v[72:75], v[120:123], v[168:171]\n"
    "s_waitcnt lgkmcnt(6)\n"
    "v_mfma_f32_16x16x32_bf16 v[168:171], v[76:79], v[124:127], v[168:171]\n"
    "s_add_u32 s97, s97, 1\n"
    "s_cmp_eq_u32 s97, 3\n"
    "s_waitcnt lgkmcnt(4)\n"
    "v_mfma_f32_16x16x32_bf16 v[172:175], v[80:83], v[120:123], v[172:175]\n"
    "s_cselect_b32 s97, 0, s97\n"
    "s_cselect_b32 s58, 4294885120, s12\n"
    "s_waitcnt lgkmcnt(2)\n"
    "v_mfma_f32_16x16x32_bf16 v[172:175], v[84:87], v[124:127], v[172:175]\n"
    "v_mfma_f32_16x16x32_bf16 v[176:179], v[56:59], v[128:131], v[176:179]\n"
    "ds_read_b128 v[120:123], v2 offset:6144\n"
    "v_mfma_f32_16x16x32_bf16 v[176:179], v[60:63], v[132:135], v[176:179]\n"
    "ds_read_b128 v[124:127], v3 offset:6144\n"
    "v_mfma_f32_16x16x32_bf16 v[180:183], v[64:67], v[128:131], v[180:183]\n"
    "v_add_u32 v4, s58, v4\n"
    "v_mfma_f32_16x16x32_bf16 v[180:183], v[68:71], v[132:135], v[180:183]\n"
    "v_add_u32 v5, s58, v5\n"
    "v_mfma_f32_16x16x32_bf16 v[184:187], v[72:75], v[128:131], v[184:187]\n"
    "v_mfma_f32_16x16x32_bf16 v[184:187], v[76:79], v[132:135], v[184:187]\n"
    "v_mfma_f32_16x16x32_bf16 v[188:191], v[80:83], v[128:131], v[188:191]\n"
    "v_mfma_f32_16x16x32_bf16 v[188:191], v[84:87], v[132:135], v[188:191]\n"
    "s_waitcnt lgkmcnt(3)\n"
    "v_mfma_f32_16x16x32_bf16 v[192:195], v[56:59], v[136:139], v[192:195]\n"
    "ds_read_b128 v[128:131], v2 offset:8192\n"
    "s_waitcnt lgkmcnt(3)\n"
    "v_mfma_f32_16x16x32_bf16 v[192:195], v[60:63], v[140:143], v[192:195]\n"
    "ds_read_b128 v[132:135], v3 offset:8192\n"
    "v_mfma_f32_16x16x32_bf16 v[196:199], v[64:67], v[136:139], v[196:199]\n"
    "v_mfma_f32_16x16x32_bf16 v[196:199], v[68:71], v[140:143], v[196:199]\n"
    "v_mfma_f32_16x16x32_bf16 v[200:203], v[72:75], v[136:139], v[200:203]\n"
    "v_mfma_f32_16x16x32_bf16 v[200:203], v[76:79], v[140:143], v[200:203]\n"
    "v_mfma_f32_16x16x32_bf16 v[204:207], v[80:83], v[136:139], v[204:207]\n"
    "v_mfma_f32_16x16x32_bf16 v[204:207], v[84:87], v[140:143], v[204:207]\n"
    "s_waitcnt lgkmcnt(3)\n"
    "v_mfma_f32_16x16x32_bf16 v[208:211], v[56:59], v[120:123], v[208:211]\n"
    "ds_read_b128 v[136:139], v2 offset:10240\n"
    "s_waitcnt lgkmcnt(3)\n"
    "v_mfma_f32_16x16x32_bf16 v[208:211], v[60:63], v[124:127], v[208:211]\n"
    "ds_read_b128 v[140:143], v3 offset:10240\n"
    "v_mfma_f32_16x16x32_bf16 v[212:215], v[64:67], v[120:123], v[212:215]\n"
    "v_add_u32 v2, s58, v2\n"
    "v_mfma_f32_16x16x32_bf16 v[212:215], v[68:71], v[124:127], v[212:215]\n"
    "v_add_u32 v3, s58, v3\n"
    "v_mfma_f32_16x16x32_bf16 v[216:219], v[72:75], v[120:123], v[216:219]\n"
    "v_mfma_f32_16x16x32_bf16 v[216:219], v[76:79], v[124:127], v[216:219]\n"
    "s_cmp_lg_u32 s61, 3\n"
    "v_mfma_f32_16x16x32_bf16 v[220:223], v[80:83], v[120:123], v[220:223]\n"
    "s_cbranch_scc1 Ldz18_%=\n"
    "s_waitcnt vmcnt(0)\n"
    "Ldz18_%=:\n"
    "v_mfma_f32_16x16x32_bf16 v[220:223], v[84:87], v[124:127], v[220:223]\n"
    "s_waitcnt lgkmcnt(0)\n"
    "s_barrier\n"
    "ds_read_b64_tr_b16 v[88:89], v4 offset:0\n"
    "ds_read_b64_tr_b16 v[90:91], v4 offset:512\n"
    "v_mfma_f32_16x16x32_bf16 v[224:227], v[56:59], v[128:131], v[224:227]\n"
    "ds_read_b128 v[120:123], v2 offset:0\n"
    "v_mfma_f32_16x16x32_bf16 v[224:227], v[60:63], v[132:135], v[224:227]\n"
    "ds_read_b128 v[124:127], v3 offset:0\n"
    "v_mfma_f32_16x16x32_bf16 v[228:231], v[64:67], v[128:131], v[228:231]\n"
    "ds_read_b64_tr_b16 v[92:93], v4 offset:4096\n"
    "ds_read_b64_tr_b16 v[94:95], v4 offset:4608\n"
    "v_mfma_f32_16x16x32_bf16 v[228:231], v[68:71], v[132:135], v[228:231]\n"
    "ds_read_b64_tr_b16 v[96:97], v5 offset:0\n"
    "ds_read_b64_tr_b16 v[98:99], v5 offset:512\n"
    "v_mfma_f32_16x16x32_bf16 v[232:235], v[72:75], v[128:131], v[232:235]\n"
    "ds_read_b64_tr_b16 v[100:101], v5 offset:4096\n"
    "ds_read_b64_tr_b16 v[102:103], v5 offset:4608\n"
    "v_mfma_f32_16x16x32_bf16 v[232:235], v[76:79], v[132:135], v[232:235]\n"
    "ds_read_b64_tr_b16 v[104:105], v4 offset:8192\n"
    "v_mfma_f32_16x16x32_bf16 v[236:239], v[80:83], v[128:131], v[236:239]\n"
    "ds_read_b64_tr_b16 v[106:107], v4 offset:8704\n"
    "v_mfma_f32_16x16x32_bf16 v[236:239], v[84:87], v[132:135], v[236:239]\n"
    "ds_read_b64_tr_b16 v[108:109], v4 offset:12288\n"
    "v_mfma_f32_16x16x32_bf16 v[240:243], v[56:59], v[136:139], v[240:243]\n"
    "ds_read_b128 v[128:131], v2 offset:2048\n"
    "v_mfma_f32_16x16x32_bf16 v[240:243], v[60:63], v[140:143], v[240:243]\n"
    "ds_read_b128 v[132:135], v3 offset:2048\n"
    "v_mfma_f32_16x16x32_bf16 v[244:247], v[64:67], v[136:139], v[244:247]\n"
    "ds_read_b64_tr_b16 v[110:111], v4 offset:12800\n"
    "v_mfma_f32_16x16x32_bf16 v[244:247], v[68:71], v[140:143], v[244:247]\n"
    "ds_read_b64_tr_b16 v[112:113], v5 offset:8192\n"
    "v_mfma_f32_16x16x32_bf16 v[248:251], v[72:75], v[136:139], v[248:251]\n"
    "ds_read_b64_tr_b16 v[114:115], v5 offset:8704\n"
    "v_mfma_f32_16x16x32_bf16 v[248:251], v[76:79], v[140:143], v[248:251]\n"
    "ds_read_b64_tr_b16 v[116:117], v5 offset:12288\n"
    "v_mfma_f32_16x16x32_bf16 v[252:255], v[80:83], v[136:139], v[252:255]\n"
    "ds_read_b64_tr_b16 v[118:119], v5 offset:12800\n"
    "v_mfma_f32_16x16x32_bf16 v[252:255], v[84:87], v[140:143], v[252:255]\n"
    "s_waitcnt lgkmcnt(15)\n"
    "v_mfma_f32_16x16x32_bf16 v[160:163], v[88:91], v[120:123], v[160:163]\n"
    "ds_read_b128 v[136:139], v2 offset:4096\n"
    "s_waitcnt lgkmcnt(15)\n"
    "v_mfma_f32_16x16x32_bf16 v[160:163], v[92:95], v[124:127], v[160:163]\n"
    "ds_read_b128 v[140:143], v3 offset:4096\n"
    "s_waitcnt lgkmcnt(14)\n"
    "v_mfma_f32_16x16x32_bf16 v[164:167], v[96:99], v[120:123], v[164:167]\n"
    "s_waitcnt lgkmcnt(12)\n"
    "v_mfma_f32_16x16x32_bf16 v[164:167], v[100:103], v[124:127], v[164:167]\n"
    "s_waitcnt lgkmcnt(10)\n"
    "v_mfma_f32_16x16x32_bf16 v[168:171], v[104:107], v[120:123], v[168:171]\n"
    "s_waitcnt lgkmcnt(6)\n"
    "v_mfma_f32_16x16x32_bf16 v[168:171], v[108:111], v[124:127], v[168:171]\n"
    "s_add_u32 s97, s97, 1\n"
    "s_cmp_eq_u32 s97, 3\n"
    "s_waitcnt lgkmcnt(4)\n"
    "v_mfma_f32_16x16x32_bf16 v[172:175], v[112:115], v[120:123], v[172:175]\n"
    "s_cselect_b32 s97, 0, s97\n"
    "s_cselect_b32 s58, 4294885120, s12\n"
    "s_waitcnt lgkmcnt(2)\n"
    "v_mfma_f32_16x16x32_bf16 v[172:175], v[116:119], v[124:127], v[172:175]\n"
    "v_mfma_f32_16x16x32_bf16 v[176:179], v[88:91], v[128:131], v[176:179]\n"
    "ds_read_b128 v[120:123], v2 offset:6144\n"
    "v_mfma_f32_16x16x32_bf16 v[176:179], v[92:95], v[132:135], v[176:179]\n"
    "ds_read_b128 v[124:127], v3 offset:6144\n"
    "v_mfma_f32_16x16x32_bf16 v[180:183], v[96:99], v[128:131], v[180:183]\n"
    "v_add_u32 v4, s58, v4\n"
    "v_mfma_f32_16x16x32_bf16 v[180:183], v[100:103], v[132:135], v[180:183]\n"
    "v_add_u32 v5, s58, v5\n"
    "v_mfma_f32_16x16x32_bf16 v[184:187], v[104:107], v[128:131], v[184:187]\n"
    "v_mfma_f32_16x16x32_bf16 v[184:187], v[108:111], v[132:135], v[184:187]\n"
    "v_mfma_f32_16x16x32_bf16 v[188:191], v[112:115], v[128:131], v[188:191]\n"
    "v_mfma_f32_16x16x32_bf16 v[188:191], v[116:119], v[132:135], v[188:191]\n"
    "s_waitcnt lgkmcnt(3)\n"
    "v_mfma_f32_16x16x32_bf16 v[192:195], v[88:91], v[136:139], v[192:195]\n"
    "ds_read_b128 v[128:131], v2 offset:8192\n"
    "s_waitcnt lgkmcnt(3)\n"
    "v_mfma_f32_16x16x32_bf16 v[192:195], v[92:95], v[140:143], v[192:195]\n"
    "ds_read_b128 v[132:135], v3 offset:8192\n"
    "v_mfma_f32_16x16x32_bf16 v[196:199], v[96:99], v[136:139], v[196:199]\n"
    "v_mfma_f32_16x16x32_bf16 v[196:199], v[100:103], v[140:143], v[196:199]\n"
    "v_mfma_f32_16x16x32_bf16 v[200:203], v[104:107], v[136:139], v[200:203]\n"
    "v_mfma_f32_16x16x32_bf16 v[200:203], v[108:111], v[140:143], v[200:203]\n"
    "v_mfma_f32_16x16x32_bf16 v[204:207], v[112:115], v[136:139], v[204:207]\n"
    "v_mfma_f32_16x16x32_bf16 v[204:207], v[116:119], v[140:143], v[204:207]\n"
    "s_waitcnt lgkmcnt(3)\n"
    "v_mfma_f32_16x16x32_bf16 v[208:211], v[88:91], v[120:123], v[208:211]\n"
    "ds_read_b128 v[136:139], v2 offset:10240\n"
    "s_waitcnt lgkmcnt(3)\n"
    "v_mfma_f32_16x16x32_bf16 v[208:211], v[92:95], v[124:127], v[208:211]\n"
    "ds_read_b128 v[140:143], v3 offset:10240\n"
    "v_mfma_f32_16x16x32_bf16 v[212:215], v[96:99], v[120:123], v[212:215]\n"
    "v_add_u32 v2, s58, v2\n"
    "v_mfma_f32_16x16x32_bf16 v[212:215], v[100:103], v[124:127], v[212:215]\n"
    "s_cmp_eq_u32 s61, 4\n"
    "v_add_u32 v3, s58, v3\n"
    "v_mfma_f32_16x16x32_bf16 v[216:219], v[104:107], v[120:123], v[216:219]\n"
    "s_cbranch_scc1 LPF_%=\n"
    "LPFb_%=:\n"
    "v_mfma_f32_16x16x32_bf16 v[216:219], v[108:111], v[124:127], v[216:219]\n"
    "v_mfma_f32_16x16x32_bf16 v[220:223], v[112:115], v[120:123], v[220:223]\n"
    "v_mfma_f32_16x16x32_bf16 v[220:223], v[116:119], v[124:127], v[220:223]\n"
    "s_waitcnt lgkmcnt(0)\n"
    "s_barrier\n"
    "ds_read_b64_tr_b16 v[56:57], v4 offset:0\n"
    "ds_read_b64_tr_b16 v[58:59], v4 offset:512\n"
    "v_mfma_f32_16x16x32_bf16 v[224:227], v[88:91], v[128:131], v[224:227]\n"
    "ds_read_b128 v[120:123], v2 offset:0\n"
    "v_mfma_f32_16x16x32_bf16 v[224:227], v[92:95], v[132:135], v[224:227]\n"
    "ds_read_b128 v[124:127], v3 offset:0\n"
    "v_mfma_f32_16x16x32_bf16 v[228:231], v[96:99], v[128:131], v[228:231]\n"
    "ds_read_b64_tr_b16 v[60:61], v4 offset:4096\n"
    "ds_read_b64_tr_b16 v[62:63], v4 offset:4608\n"
    "v_mfma_f32_16x16x32_bf16 v[228:231], v[100:103], v[132:135], v[228:231]\n"
    "ds_read_b64_tr_b16 v[64:65], v5 offset:0\n"
    "ds_read_b64_tr_b16 v[66:67], v5 offset:512\n"
    "v_mfma_f32_16x16x32_bf16 v[232:235], v[104:107], v[128:131], v[232:235]\n"
    "ds_read_b64_tr_b16 v[68:69], v5 offset:4096\n"
    "ds_read_b64_tr_b16 v[70:71], v5 offset:4608\n"
    "v_mfma_f32_16x16x32_bf16 v[232:235], v[108:111], v[132:135], v[232:235]\n"
    "ds_read_b64_tr_b16 v[72:73], v4 offset:8192\n"
    "v_mfma_f32_16x16x32_bf16 v[236:239], v[112:115], v[128:131], v[236:239]\n"
    "ds_read_b64_tr_b16 v[74:75], v4 offset:8704\n"
    "v_mfma_f32_16x16x32_bf16 v[236:239], v[116:119], v[132:135], v[236:239]\n"
    "ds_read_b64_tr_b16 v[76:77], v4 offset:12288\n"
    "v_mfma_f32_16x16x32_bf16 v[240:243], v[88:91], v[136:139], v[240:243]\n"
    "ds_read_b128 v[128:131], v2 offset:2048\n"
    "v_mfma_f32_16x16x32_bf16 v[240:243], v[92:95], v[140:143], v[240:243]\n"
    "ds_read_b128 v[132:135], v3 offset:2048\n"
    "v_mfma_f32_16x16x32_bf16 v[244:247], v[96:99], v[136:139], v[244:247]\n"
    "ds_read_b64_tr_b16 v[78:79], v4 offset:12800\n"
    "v_mfma_f32_16x16x32_bf16 v[244:247], v[100:103], v[140:143], v[244:247]\n"
    "ds_read_b64_tr_b16 v[80:81], v5 offset:8192\n"
    "v_mfma_f32_16x16x32_bf16 v[248:251], v[104:107], v[136:139], v[248:251]\n"
    "ds_read_b64_tr_b16 v[82:83], v5 offset:8704\n"
    "v_mfma_f32_16x16x32_bf16 v[248:251], v[108:111], v[140:143], v[248:251]\n"
    "ds_read_b64_tr_b16 v[84:85], v5 offset:12288\n"
    "v_mfma_f32_16x16x32_bf16 v[252:255], v[112:115], v[136:139], v[252:255]\n"
    "ds_read_b64_tr_b16 v[86:87], v5 offset:12800\n"
    "s_sub_u32 s61, s61, 1\n"
    "s_cmp_lg_u32 s61, 0\n"
    "v_mfma_f32_16x16x32_bf16 v[252:255], v[116:119], v[140:143], v[252:255]\n"
    "s_cbranch_scc1 LCm_%=\n"
    "s_waitcnt lgkmcnt(15)\n"
    "v_mfma_f32_16x16x32_bf16 v[160:163], v[56:59], v[120:123], v[160:163]\n"
    "ds_read_b128 v[136:139], v2 offset:4096\n"
    "s_waitcnt lgkmcnt(15)\n"
    "v_mfma_f32_16x16x32_bf16 v[160:163], v[60:63], v[124:127], v[160:163]\n"
    "ds_read_b128 v[140:143], v3 offset:4096\n"
    "s_waitcnt lgkmcnt(14)\n"
    "v_mfma_f32_16x16x32_bf16 v[164:167], v[64:67], v[120:123], v[164:167]\n"
    "s_waitcnt lgkmcnt(12)\n"
    "v_mfma_f32_16x16x32_bf16 v[164:167], v[68:71], v[124:127], v[164:167]\n"
    "s_waitcnt lgkmcnt(10)\n"
    "v_mfma_f32_16x16x32_bf16 v[168:171], v[72:75], v[120:123], v[168:171]\n"
    "s_waitcnt lgkmcnt(6)\n"
    "v_mfma_f32_16x16x32_bf16 v[168:171], v[76:79], v[124:127], v[168:171]\n"
    "s_add_u32 s97, s97, 1\n"
    "s_cmp_eq_u32 s97, 3\n"
    "s_waitcnt lgkmcnt(4)\n"
    "v_mfma_f32_16x16x32_bf16 v[172:175], v[80:83], v[120:123], v[172:175]\n"
    "s_cselect_b32 s97, 0, s97\n"
    "s_cselect_b32 s58, 4294885120, s12\n"
    "s_waitcnt lgkmcnt(2)\n"
    "v_mfma_f32_16x16x32_bf16 v[172:175], v[84:87], v[124:127], v[172:175]\n"
    "v_mfma_f32_16x16x32_bf16 v[176:179], v[56:59], v[128:131], v[176:179]\n"
    "ds_read_b128 v[120:123], v2 offset:6144\n"
    "v_mfma_f32_16x16x32_bf16 v[176:179], v[60:63], v[132:135], v[176:179]\n"
    "ds_read_b128 v[124:127], v3 offset:6144\n"
    "v_mfma_f32_16x16x32_bf16 v[180:183], v[64:67], v[128:131], v[180:183]\n"
    "v_add_u32 v4, s58, v4\n"
    "v_mfma_f32_16x16x32_bf16 v[180:183], v[68:71], v[132:135], v[180:183]\n"
    "v_add_u32 v5, s58, v5\n"
    "v_mfma_f32_16x16x32_bf16 v[184:187], v[72:75], v[128:131], v[184:187]\n"
    "v_mfma_f32_16x16x32_bf16 v[184:187], v[76:79], v[132:135], v[184:187]\n"
    "v_mfma_f32_16x16x32_bf16 v[188:191], v[80:83], v[128:131], v[188:191]\n"
    "v_mfma_f32_16x16x32_bf16 v[188:191], v[84:87], v[132:135], v[188:191]\n"
    "s_waitcnt lgkmcnt(3)\n"
    "v_mfma_f32_16x16x32_bf16 v[192:195], v[56:59], v[136:139], v[192:195]\n"
    "ds_read_b128 v[128:131], v2 offset:8192\n"
    "s_waitcnt lgkmcnt(3)\n"
    "v_mfma_f32_16x16x32_bf16 v[192:195], v[60:63], v[140:143], v[192:195]\n"
    "ds_read_b128 v[132:135], v3 offset:8192\n"
    "v_mfma_f32_16x16x32_bf16 v[196:199], v[64:67], v[136:139], v[196:199]\n"
    "v_mfma_f32_16x16x32_bf16 v[196:199], v[68:71], v[140:143], v[196:199]\n"
    "v_mfma_f32_16x16x32_bf16 v[200:203], v[72:75], v[136:139], v[200:203]\n"
    "v_mfma_f32_16x16x32_bf16 v[200:203], v[76:79], v[140:143], v[200:203]\n"
    "v_mfma_f32_16x16x32_bf16 v[204:207], v[80:83], v[136:139], v[204:207]\n"
    "v_mfma_f32_16x16x32_bf16 v[204:207], v[84:87], v[140:143], v[204:207]\n"
    "s_waitcnt lgkmcnt(3)\n"
    "v_mfma_f32_16x16x32_bf16 v[208:211], v[56:59], v[120:123], v[208:211]\n"
    "ds_read_b128 v[136:139], v2 offset:10240\n"
    "s_waitcnt lgkmcnt(3)\n"
    "v_mfma_f32_16x16x32_bf16 v[208:211], v[60:63], v[124:127], v[208:211]\n"
    "ds_read_b128 v[140:143], v3 offset:10240\n"
    "v_mfma_f32_16x16x32_bf16 v[212:215], v[64:67], v[120:123], v[212:215]\n"
    "v_add_u32 v2, s58, v2\n"
    "v_mfma_f32_16x16x32_bf16 v[212:215], v[68:71], v[124:127], v[212:215]\n"
    "v_add_u32 v3, s58, v3\n"
    "v_mfma_f32_16x16x32_bf16 v[216:219], v[72:75], v[120:123], v[216:219]\n"
    "v_mfma_f32_16x16x32_bf16 v[216:219], v[76:79], v[124:127], v[216:219]\n"
    "v_mfma_f32_16x16x32_bf16 v[220:223], v[80:83], v[120:123], v[220:223]\n"
    "v_mfma_f32_16x16x32_bf16 v[220:223], v[84:87], v[124:127], v[220:223]\n"
    "s_waitcnt lgkmcnt(0)\n"
    "s_barrier\n"
    "ds_read_b64_tr_b16 v[88:89], v4 offset:0\n"
    "ds_read_b64_tr_b16 v[90:91], v4 offset:512\n"
    "v_mfma_f32_16x16x32_bf16 v[224:227], v[56:59], v[128:131], v[224:227]\n"
    "ds_read_b128 v[120:123], v2 offset:0\n"
    "v_mfma_f32_16x16x32_bf16 v[224:227], v[60:63], v[132:135], v[224:227]\n"
    "ds_read_b128 v[124:127], v3 offset:0\n"
    "v_mfma_f32_16x16x32_bf16 v[228:231], v[64:67], v[128:131], v[228:231]\n"
    "ds_read_b64_tr_b16 v[92:93], v4 offset:4096\n"
    "ds_read_b64_tr_b16 v[94:95], v4 offset:4608\n"
    "v_mfma_f32_16x16x32_bf16 v[228:231], v[68:71], v[132:135], v[228:231]\n"
    "ds_read_b64_tr_b16 v[96:97], v5 offset:0\n"
    "ds_read_b64_tr_b16 v[98:99], v5 offset:512\n"
    "v_mfma_f32_16x16x32_bf16 v[232:235], v[72:75], v[128:131], v[232:235]\n"
    "ds_read_b64_tr_b16 v[100:101], v5 offset:4096\n"
    "ds_read_b64_tr_b16 v[102:103], v5 offset:4608\n"
    "v_mfma_f32_16x16x32_bf16 v[232:235], v[76:79], v[132:135], v[232:235]\n"
    "ds_read_b64_tr_b16 v[104:105], v4 offset:8192\n"
    "v_mfma_f32_16x16x32_bf16 v[236:239], v[80:83], v[128:131], v[236:239]\n"
    "ds_read_b64_tr_b16 v[106:107], v4 offset:8704\n"
    "v_mfma_f32_16x16x32_bf16 v[236:239], v[84:87], v[132:135], v[236:239]\n"
    "ds_read_b64_tr_b16 v[108:109], v4 offset:12288\n"
    "v_mfma_f32_16x16x32_bf16 v[240:243], v[56:59], v[136:139], v[240:243]\n"
    "ds_read_b128 v[128:131], v2 offset:2048\n"
    "v_mfma_f32_16x16x32_bf16 v[240:243], v[60:63], v[140:143], v[240:243]\n"
    "ds_read_b128 v[132:135], v3 offset:2048\n"
    "v_mfma_f32_16x16x32_bf16 v[244:247], v[64:67], v[136:139], v[244:247]\n"
    "ds_read_b64_tr_b16 v[110:111], v4 offset:12800\n"
    "v_mfma_f32_16x16x32_bf16 v[244:247], v[68:71], v[140:143], v[244:247]\n"
    "ds_read_b64_tr_b16 v[112:113], v5 offset:8192\n"
    "v_mfma_f32_16x16x32_bf16 v[248:251], v[72:75], v[136:139], v[248:251]\n"
    "ds_read_b64_tr_b16 v[114:115], v5 offset:8704\n"
    "v_mfma_f32_16x16x32_bf16 v[248:251], v[76:79], v[140:143], v[248:251]\n"
    "ds_read_b64_tr_b16 v[116:117], v5 offset:12288\n"
    "v_mfma_f32_16x16x32_bf16 v[252:255], v[80:83], v[136:139], v[252:255]\n"
    "ds_read_b64_tr_b16 v[118:119], v5 offset:12800\n"
    "v_mfma_f32_16x16x32_bf16 v[252:255], v[84:87], v[140:143], v[252:255]\n"
    "s_waitcnt lgkmcnt(15)\n"
    "v_mfma_f32_16x16x32_bf16 v[160:163], v[88:91], v[120:123], v[160:163]\n"
    "ds_read_b128 v[136:139], v2 offset:4096\n"
    "s_waitcnt lgkmcnt(15)\n"
    "v_mfma_f32_16x16x32_bf16 v[160:163], v[92:95], v[124:127], v[160:163]\n"
    "ds_read_b128 v[140:143], v3 offset:4096\n"
    "s_waitcnt lgkmcnt(14)\n"
    "v_mfma_f32_16x16x32_bf16 v[164:167], v[96:99], v[120:123], v[164:167]\n"
    "ds_read_b64 v[158:159], v158\n"
    "s_waitcnt lgkmcnt(13)\n"
    "v_mfma_f32_16x16x32_bf16 v[164:167], v[100:103], v[124:127], v[164:167]\n"
    "s_waitcnt lgkmcnt(11)\n"
    "v_mfma_f32_16x16x32_bf16 v[168:171], v[104:107], v[120:123], v[168:171]\n"
    "s_waitcnt lgkmcnt(7)\n"
    "v_mfma_f32_16x16x32_bf16 v[168:171], v[108:111], v[124:127], v[168:171]\n"
    "s_waitcnt lgkmcnt(5)\n"
    "v_mfma_f32_16x16x32_bf16 v[172:175], v[112:115], v[120:123], v[172:175]\n"
    "s_waitcnt lgkmcnt(0)\n"
    "v_add_co_u32 v158, vcc, 1, v158\n"
    "v_mfma_f32_16x16x32_bf16 v[172:175], v[116:119], v[124:127], v[172:175]\n"
    "s_nop 0\n"
    "v_addc_co_u32 v159, vcc, 0, v159, vcc\n"
    "v_mfma_f32_16x16x32_bf16 v[176:179], v[88:91], v[128:131], v[176:179]\n"
    "ds_read_b128 v[120:123], v2 offset:6144\n"
    "v_mfma_f32_16x16x32_bf16 v[176:179], v[92:95], v[132:135], v[176:179]\n"
    "ds_read_b128 v[124:127], v3 offset:6144\n"
    "v_mfma_f32_16x16x32_bf16 v[180:183], v[96:99], v[128:131], v[180:183]\n"
    "v_mul_f32 v24, s54, v160\n"
    "v_mul_f32 v25, s54, v161\n"
    "v_mfma_f32_16x16x32_bf16 v[180:183], v[100:103], v[132:135], v[180:183]\n"
    "v_mul_f32 v26, s54, v162\n"
    "v_mul_f32 v27, s54, v163\n"
    "v_mfma_f32_16x16x32_bf16 v[184:187], v[104:107], v[128:131], v[184:187]\n"
    "v_mul_f32 v28, s54, v164\n"
    "v_mul_f32 v29, s54, v165\n"
    "v_mfma_f32_16x16x32_bf16 v[184:187], v[108:111], v[132:135], v[184:187]\n"
    "v_mul_f32 v30, s54, v166\n"
    "v_mul_f32 v31, s54, v167\n"
    "v_mfma_f32_16x16x32_bf16 v[188:191], v[112:115], v[128:131], v[188:191]\n"
    "v_exp_f32 v24, v24\n"
    "v_mfma_f32_16x16x32_bf16 v[188:191], v[116:119], v[132:135], v[188:191]\n"
    "v_exp_f32 v25, v25\n"
    "v_mfma_f32_16x16x32_bf16 v[192:195], v[88:91], v[136:139], v[192:195]\n"
    "ds_read_b128 v[128:131], v2 offset:8192\n"
    "v_mfma_f32_16x16x32_bf16 v[192:195], v[92:95], v[140:143], v[192:195]\n"
    "ds_read_b128 v[132:135], v3 offset:8192\n"
    "v_mfma_f32_16x16x32_bf16 v[196:199], v[96:99], v[136:139], v[196:199]\n"
    "v_exp_f32 v26, v26\n"
    "v_mfma_f32_16x16x32_bf16 v[196:199], v[100:103], v[140:143], v[196:199]\n"
    "v_exp_f32 v27, v27\n"
    "v_mfma_f32_16x16x32_bf16 v[200:203], v[104:107], v[136:139], v[200:203]\n"
    "v_exp_f32 v28, v28\n"
    "v_mfma_f32_16x16x32_bf16 v[200:203], v[108:111], v[140:143], v[200:203]\n"
    "v_exp_f32 v29, v29\n"
    "v_mfma_f32_16x16x32_bf16 v[204:207], v[112:115], v[136:139], v[204:207]\n"
    "v_exp_f32 v30, v30\n"
    "v_mfma_f32_16x16x32_bf16 v[204:207], v[116:119], v[140:143], v[204:207]\n"
    "v_exp_f32 v31, v31\n"
    "s_waitcnt lgkmcnt(3)\n"
    "v_mfma_f32_16x16x32_bf16 v[208:211], v[88:91], v[120:123], v[208:211]\n"
    "ds_read_b128 v[136:139], v2 offset:10240\n"
    "v_add_f32 v24, 1.0, v24\n"
    "s_waitcnt lgkmcnt(3)\n"
    "v_mfma_f32_16x16x32_bf16 v[208:211], v[92:95], v[124:127], v[208:211]\n"
    "ds_read_b128 v[140:143], v3 offset:10240\n"
    "v_add_f32 v25, 1.0, v25\n"
    "v_mfma_f32_16x16x32_bf16 v[212:215], v[96:99], v[120:123], v[212:215]\n"
    "v_add_f32 v26, 1.0, v26\n"
    "v_add_f32 v27, 1.0, v27\n"
    "v_mfma_f32_16x16x32_bf16 v[212:215], v[100:103], v[124:127], v[212:215]\n"
    "v_add_f32 v28, 1.0, v28\n"
    "v_add_f32 v29, 1.0, v29\n"
    "v_mfma_f32_16x16x32_bf16 v[216:219], v[104:107], v[120:123], v[216:219]\n"
    "v_add_f32 v30, 1.0, v30\n"
    "v_add_f32 v31, 1.0, v31\n"
    "v_mfma_f32_16x16x32_bf16 v[216:219], v[108:111], v[124:127], v[216:219]\n"
    "v_rcp_f32 v24, v24\n"
    "v_mfma_f32_16x16x32_bf16 v[220:223], v[112:115], v[120:123], v[220:223]\n"
    "v_rcp_f32 v25, v25\n"
    "v_mfma_f32_16x16x32_bf16 v[220:223], v[116:119], v[124:127], v[220:223]\n"
    "v_rcp_f32 v26, v26\n"
    "s_waitcnt lgkmcnt(3)\n"
    "v_mfma_f32_16x16x32_bf16 v[224:227], v[88:91], v[128:131], v[224:227]\n"
    "v_rcp_f32 v27, v27\n"
    "s_waitcnt lgkmcnt(2)\n"
    "v_mfma_f32_16x16x32_bf16 v[224:227], v[92:95], v[132:135], v[224:227]\n"
    "v_rcp_f32 v28, v28\n"
    "v_mfma_f32_16x16x32_bf16 v[228:231], v[96:99], v[128:131], v[228:231]\n"
    "v_rcp_f32 v29, v29\n"
    "v_mfma_f32_16x16x32_bf16 v[228:231], v[100:103], v[132:135], v[228:231]\n"
    "v_rcp_f32 v30, v30\n"
    "v_mfma_f32_16x16x32_bf16 v[232:235], v[104:107], v[128:131], v[232:235]\n"
    "v_rcp_f32 v31, v31\n"
    "v_mfma_f32_16x16x32_bf16 v[232:235], v[108:111], v[132:135], v[232:235]\n"
    "v_mul_f32 v24, v24, v168\n"
    "v_mul_f32 v25, v25, v169\n"
    "v_mfma_f32_16x16x32_bf16 v[236:239], v[112:115], v[128:131], v[236:239]\n"
    "v_mul_f32 v26, v26, v170\n"
    "v_mul_f32 v27, v27, v171\n"
    "v_mfma_f32_16x16x32_bf16 v[236:239], v[116:119], v[132:135], v[236:239]\n"
    "v_mul_f32 v28, v28, v172\n"
    "v_mul_f32 v29, v29, v173\n"
    "s_waitcnt lgkmcnt(1)\n"
    "v_mfma_f32_16x16x32_bf16 v[240:243], v[88:91], v[136:139], v[240:243]\n"
    "v_mul_f32 v30, v30, v174\n"
    "v_mul_f32 v31, v31, v175\n"
    "s_waitcnt lgkmcnt(0)\n"
    "v_mfma_f32_16x16x32_bf16 v[240:243], v[92:95], v[140:143], v[240:243]\n"
    "v_cvt_pk_bf16_f32 v160, v24, v25\n"
    "v_cvt_pk_bf16_f32 v161, v26, v27\n"
    "v_mfma_f32_16x16x32_bf16 v[244:247], v[96:99], v[136:139], v[244:247]\n"
    "v_cvt_pk_bf16_f32 v162, v28, v29\n"
    "v_cvt_pk_bf16_f32 v163, v30, v31\n"
    "v_mfma_f32_16x16x32_bf16 v[244:247], v[100:103], v[140:143], v[244:247]\n"
    "buffer_store_dwordx4 v[160:163], v20, s[32:35], s60 offen offset:0 sc1\n"
    "s_add_u32 s60, s60, s59\n"
    "v_mfma_f32_16x16x32_bf16 v[248:251], v[104:107], v[136:139], v[248:251]\n"
    "v_mfma_f32_16x16x32_bf16 v[248:251], v[108:111], v[140:143], v[248:251]\n"
    "v_mfma_f32_16x16x32_bf16 v[252:255], v[112:115], v[136:139], v[252:255]\n"
    "v_mfma_f32_16x16x32_bf16 v[252:255], v[116:119], v[140:143], v[252:255]\n"
    "LCepi_%=:\n"
    "v_pk_mul_f32 v[40:41], v[176:177], s[54:55]\n"
    "v_pk_mul_f32 v[42:43], v[178:179], s[54:55]\n"
    "v_pk_mul_f32 v[44:45], v[180:181], s[54:55]\n"
    "v_pk_mul_f32 v[46:47], v[182:183], s[54:55]\n"
    "v_exp_f32 v40, v40\n"
    "v_exp_f32 v41, v41\n"
    "v_exp_f32 v42, v42\n"
    "v_exp_f32 v43, v43\n"
    "v_exp_f32 v44, v44\n"
    "v_exp_f32 v45, v45\n"
    "v_exp_f32 v46, v46\n"
    "v_exp_f32 v47, v47\n"
    "v_pk_add_f32 v[40:41], v[40:41], 1.0 op_sel_hi:[1,0]\n"
    "v_pk_add_f32 v[42:43], v[42:43], 1.0 op_sel_hi:[1,0]\n"
    "v_pk_add_f32 v[44:45], v[44:45], 1.0 op_sel_hi:[1,0]\n"
    "v_pk_add_f32 v[46:47], v[46:47], 1.0 op_sel_hi:[1,0]\n"
    "v_rcp_f32 v40, v40\n"
    "v_rcp_f32 v41, v41\n"
    "v_rcp_f32 v42, v42\n"
    "v_rcp_f32 v43, v43\n"
    "v_rcp_f32 v44, v44\n"
    "v_rcp_f32 v45, v45\n"
    "v_rcp_f32 v46, v46\n"
    "v_rcp_f32 v47, v47\n"
    "v_pk_mul_f32 v[40:41], v[40:41], v[184:185]\n"
    "v_pk_mul_f32 v[42:43], v[42:43], v[186:187]\n"
    "v_pk_mul_f32 v[44:45], v[44:45], v[188:189]\n"
    "v_pk_mul_f32 v[46:47], v[46:47], v[190:191]\n"
    "v_cvt_pk_bf16_f32 v176, v40, v41\n"
    "v_cvt_pk_bf16_f32 v177, v42, v43\n"
    "v_cvt_pk_bf16_f32 v178, v44, v45\n"
    "v_cvt_pk_bf16_f32 v179, v46, v47\n"
    "buffer_store_dwordx4 v[176:179], v20, s[32:35], s60 offen offset:0 sc1\n"
    "s_add_u32 s60, s60, s59\n"
    "v_pk_mul_f32 v[24:25], v[192:193], s[54:55]\n"
    "v_pk_mul_f32 v[26:27], v[194:195], s[54:55]\n"
    "v_pk_mul_f32 v[28:29], v[196:197], s[54:55]\n"
    "v_pk_mul_f32 v[30:31], v[198:199], s[54:55]\n"
    "v_exp_f32 v24, v24\n"
    "v_exp_f32 v25, v25\n"
    "v_exp_f32 v26, v26\n"
    "v_exp_f32 v27, v27\n"
    "v_exp_f32 v28, v28\n"
    "v_exp_f32 v29, v29\n"
    "v_exp_f32 v30, v30\n"
    "v_exp_f32 v31, v31\n"
    "v_pk_add_f32 v[24:25], v[24:25], 1.0 op_sel_hi:[1,0]\n"
    "v_pk_add_f32 v[26:27], v[26:27], 1.0 op_sel_hi:[1,0]\n"
    "v_pk_add_f32 v[28:29], v[28:29], 1.0 op_sel_hi:[1,0]\n"
    "v_pk_add_f32 v[30:31], v[30:31], 1.0 op_sel_hi:[1,0]\n"
    "v_rcp_f32 v24, v24\n"
    "v_rcp_f32 v25, v25\n"
    "v_rcp_f32 v26, v26\n"
    "v_rcp_f32 v27, v27\n"
    "v_rcp_f32 v28, v28\n"
    "v_rcp_f32 v29, v29\n"
    "v_rcp_f32 v30, v30\n"
    "v_rcp_f32 v31, v31\n"
    "v_pk_mul_f32 v[24:25], v[24:25], v[200:201]\n"
    "v_pk_mul_f32 v[26:27], v[26:27], v[202:203]\n"
    "v_pk_mul_f32 v[28:29], v[28:29], v[204:205]\n"
    "v_pk_mul_f32 v[30:31], v[30:31], v[206:207]\n"
    "v_cvt_pk_bf16_f32 v192, v24, v25\n"
    "v_cvt_pk_bf16_f32 v193, v26, v27\n"
    "v_cvt_pk_bf16_f32 v194, v28, v29\n"
    "v_cvt_pk_bf16_f32 v195, v30, v31\n"
    "buffer_store_dwordx4 v[192:195], v20, s[32:35], s60 offen offset:0 sc1\n"
    "s_add_u32 s60, s60, s13\n"
    "v_pk_mul_f32 v[40:41], v[208:209], s[54:55]\n"
    "v_pk_mul_f32 v[42:43], v[210:211], s[54:55]\n"
    "v_pk_mul_f32 v[44:45], v[212:213], s[54:55]\n"
    "v_pk_mul_f32 v[46:47], v[214:215], s[54:55]\n"
    "v_exp_f32 v40, v40\n"
    "v_exp_f32 v41, v41\n"
    "v_exp_f32 v42, v42\n"
    "v_exp_f32 v43, v43\n"
    "v_exp_f32 v44, v44\n"
    "v_exp_f32 v45, v45\n"
    "v_exp_f32 v46, v46\n"
    "v_exp_f32 v47, v47\n"
    "v_pk_add_f32 v[40:41], v[40:41], 1.0 op_sel_hi:[1,0]\n"
    "v_pk_add_f32 v[42:43], v[42:43], 1.0 op_sel_hi:[1,0]\n"
    "v_pk_add_f32 v[44:45], v[44:45], 1.0 op_sel_hi:[1,0]\n"
    "v_pk_add_f32 v[46:47], v[46:47], 1.0 op_sel_hi:[1,0]\n"
    "v_rcp_f32 v40, v40\n"
    "v_rcp_f32 v41, v41\n"
    "v_rcp_f32 v42, v42\n"
    "v_rcp_f32 v43, v43\n"
    "v_rcp_f32 v44, v44\n"
    "v_rcp_f32 v45, v45\n"
    "v_rcp_f32 v46, v46\n"
    "v_rcp_f32 v47, v47\n"
    "v_pk_mul_f32 v[40:41], v[40:41], v[216:217]\n"
    "v_pk_mul_f32 v[42:43], v[42:43], v[218:219]\n"
    "v_pk_mul_f32 v[44:45], v[44:45], v[220:221]\n"
    "v_pk_mul_f32 v[46:47], v[46:47], v[222:223]\n"
    "v_cvt_pk_bf16_f32 v208, v40, v41\n"
    "v_cvt_pk_bf16_f32 v209, v42, v43\n"
    "v_cvt_pk_bf16_f32 v210, v44, v45\n"
    "v_cvt_pk_bf16_f32 v211, v46, v47\n"
    "buffer_store_dwordx4 v[208:211], v20, s[32:35], s60 offen offset:0 sc1\n"
    "s_add_u32 s60, s60, s59\n"
    "v_pk_mul_f32 v[24:25], v[224:225], s[54:55]\n"
    "v_pk_mul_f32 v[26:27], v[226:227], s[54:55]\n"
    "v_pk_mul_f32 v[28:29], v[228:229], s[54:55]\n"
    "v_pk_mul_f32 v[30:31], v[230:231], s[54:55]\n"
    "v_exp_f32 v24, v24\n"
    "v_exp_f32 v25, v25\n"
    "v_exp_f32 v26, v26\n"
    "v_exp_f32 v27, v27\n"
    "v_exp_f32 v28, v28\n"
    "v_exp_f32 v29, v29\n"
    "v_exp_f32 v30, v30\n"
    "v_exp_f32 v31, v31\n"
    "v_pk_add_f32 v[24:25], v[24:25], 1.0 op_sel_hi:[1,0]\n"
    "v_pk_add_f32 v[26:27], v[26:27], 1.0 op_sel_hi:[1,0]\n"
    "v_pk_add_f32 v[28:29], v[28:29], 1.0 op_sel_hi:[1,0]\n"
    "v_pk_add_f32 v[30:31], v[30:31], 1.0 op_sel_hi:[1,0]\n"
    "v_rcp_f32 v24, v24\n"
    "v_rcp_f32 v25, v25\n"
    "v_rcp_f32 v26, v26\n"
    "v_rcp_f32 v27, v27\n"
    "v_rcp_f32 v28, v28\n"
    "v_rcp_f32 v29, v29\n"
    "v_rcp_f32 v30, v30\n"
    "v_rcp_f32 v31, v31\n"
    "v_pk_mul_f32 v[24:25], v[24:25], v[232:233]\n"
    "v_pk_mul_f32 v[26:27], v[26:27], v[234:235]\n"
    "v_pk_mul_f32 v[28:29], v[28:29], v[236:237]\n"
    "v_pk_mul_f32 v[30:31], v[30:31], v[238:239]\n"
    "v_cvt_pk_bf16_f32 v224, v24, v25\n"
    "v_cvt_pk_bf16_f32 v225, v26, v27\n"
    "v_cvt_pk_bf16_f32 v226, v28, v29\n"
    "v_cvt_pk_bf16_f32 v227, v30, v31\n"
    "buffer_store_dwordx4 v[224:227], v20, s[32:35], s60 offen offset:0 sc1\n"
    "s_add_u32 s60, s60, s59\n"
    "v_pk_mul_f32 v[40:41], v[240:241], s[54:55]\n"
    "v_pk_mul_f32 v[42:43], v[242:243], s[54:55]\n"
    "v_pk_mul_f32 v[44:45], v[244:245], s[54:55]\n"
    "v_pk_mul_f32 v[46:47], v[246:247], s[54:55]\n"
    "v_exp_f32 v40, v40\n"
    "v_exp_f32 v41, v41\n"
    "v_exp_f32 v42, v42\n"
    "v_exp_f32 v43, v43\n"
    "v_exp_f32 v44, v44\n"
    "v_exp_f32 v45, v45\n"
    "v_exp_f32 v46, v46\n"
    "v_exp_f32 v47, v47\n"
    "v_pk_add_f32 v[40:41], v[40:41], 1.0 op_sel_hi:[1,0]\n"
    "v_pk_add_f32 v[42:43], v[42:43], 1.0 op_sel_hi:[1,0]\n"
    "v_pk_add_f32 v[44:45], v[44:45], 1.0 op_sel_hi:[1,0]\n"
    "v_pk_add_f32 v[46:47], v[46:47], 1.0 op_sel_hi:[1,0]\n"
    "v_rcp_f32 v40, v40\n"
    "v_rcp_f32 v41, v41\n"
    "v_rcp_f32 v42, v42\n"
    "v_rcp_f32 v43, v43\n"
    "v_rcp_f32 v44, v44\n"
    "v_rcp_f32 v45, v45\n"
    "v_rcp_f32 v46, v46\n"
    "v_rcp_f32 v47, v47\n"
    "v_pk_mul_f32 v[40:41], v[40:41], v[248:249]\n"
    "v_pk_mul_f32 v[42:43], v[42:43], v[250:251]\n"
    "v_pk_mul_f32 v[44:45], v[44:45], v[252:253]\n"
    "v_pk_mul_f32 v[46:47], v[46:47], v[254:255]\n"
    "v_cvt_pk_bf16_f32 v240, v40, v41\n"
    "v_cvt_pk_bf16_f32 v241, v42, v43\n"
    "v_cvt_pk_bf16_f32 v242, v44, v45\n"
    "v_cvt_pk_bf16_f32 v243, v46, v47\n"
    "buffer_store_dwordx4 v[240:243], v20, s[32:35], s60 offen offset:0 sc1\n"
    "LCpub_%=:\n"
    "v_mov_b32 v18, 2\n"
    "ds_write_b32 v17, v18 offset:16\n"
    "s_lshl_b32 s60, s52, 11\n"
    "s_mov_b64 exec, 0xffff\n"
    "s_waitcnt vmcnt(0)\n"
    "buffer_store_dwordx2 v[158:159], v157, s[40:43], 0 offen sc1\n"
    "s_mov_b64 exec, -1\n"
    "v_mov_b32 v18, 3\n"
    "ds_write_b32 v17, v18 offset:16\n"
    "s_mov_b32 s3, 0\n"
    "Lsp19_%=:\n"
    "ds_read_b32 v19, v151 offset:32\n"
    "s_waitcnt lgkmcnt(0)\n"
    "v_cmp_gt_u32 vcc, 3, v19\n"
    "s_add_u32 s3, s3, 1\n"
    "s_cbranch_vccz Lsq20_%=\n"
    "s_cmp_lt_u32 s3, 8388608\n"
    "s_cbranch_scc1 Lsp19_%=\n"
    "Lsq20_%=:\n"
    "ds_read_b128 v[24:27], v146 offset:0\n"
    "ds_read_b128 v[44:47], v146 offset:10240\n"
    "ds_read_b128 v[28:31], v146 offset:1024\n"
    "ds_read_b128 v[48:51], v146 offset:11264\n"
    "ds_read_b128 v[32:35], v146 offset:2048\n"
    "ds_read_b128 v[52:55], v146 offset:12288\n"
    "ds_read_b128 v[36:39], v146 offset:3072\n"
    "ds_read_b128 v[56:59], v146 offset:13312\n"
    "ds_read_b128 v[40:43], v146 offset:4096\n"
    "ds_read_b128 v[60:63], v146 offset:14336\n"
    "ds_read_b128 v[64:67], v147 offset:0\n"
    "ds_read_b128 v[84:87], v147 offset:10240\n"
    "ds_read_b128 v[68:71], v147 offset:1024\n"
    "ds_read_b128 v[88:91], v147 offset:11264\n"
    "ds_read_b128 v[72:75], v147 offset:2048\n"
    "ds_read_b128 v[92:95], v147 offset:12288\n"
    "ds_read_b128 v[76:79], v147 offset:3072\n"
    "ds_read_b128 v[96:99], v147 offset:13312\n"
    "ds_read_b128 v[80:83], v147 offset:4096\n"
    "ds_read_b128 v[100:103], v147 offset:14336\n"
    "ds_read_b128 v[104:107], v147 offset:20480\n"
    "ds_read_b128 v[124:127], v147 offset:30720\n"
    "ds_read_b128 v[108:111], v147 offset:21504\n"
    "ds_read_b128 v[128:131], v147 offset:31744\n"
    "ds_read_b128 v[112:115], v147 offset:22528\n"
    "ds_read_b128 v[132:135], v147 offset:32768\n"
    "ds_read_b128 v[116:119], v147 offset:23552\n"
    "ds_read_b128 v[136:139], v147 offset:33792\n"
    "ds_read_b128 v[120:123], v147 offset:24576\n"
    "ds_read_b128 v[140:143], v147 offset:34816\n"
    "ds_write_b32 v17, v18 offset:48\n"
    "s_sleep 16\n"
    "s_mov_b32 s3, 0\n"
    "LCpoll_%=:\n"
    "buffer_load_dwordx2 v[12:13], v150, s[40:43], 0 offen sc1\n"
    "s_waitcnt vmcnt(0)\n"
    "v_cmp_ne_u64 vcc, v[158:159], v[12:13]\n"
    "s_add_u32 s3, s3, 1\n"
    "s_cbranch_vccz LCgo_%=\n"
    "s_cmp_lt_u32 s3, 262144\n"
    "s_sleep 1\n"
    "s_cbranch_scc1 LCpoll_%=\n"
    "LCgo_%=:\n"
    "buffer_load_dwordx4 v[200:203], v144, s[32:35], s60 offen offset:0 sc1\n"
    "buffer_load_dwordx4 v[208:211], v144, s[32:35], s60 offen offset:1024 sc1\n"
    "buffer_load_dwordx4 v[204:207], v145, s[32:35], s60 offen offset:0 sc1\n"
    "buffer_load_dwordx4 v[212:215], v145, s[32:35], s60 offen offset:1024 sc1\n"
    "s_add_u32 s60, s60, 2048\n"
    "s_cmp_eq_u32 s60, 24576\n"
    "s_cselect_b32 s60, 0, s60\n"
    "buffer_load_dwordx4 v[216:219], v144, s[32:35], s60 offen offset:0 sc1\n"
    "buffer_load_dwordx4 v[224:227], v144, s[32:35], s60 offen offset:1024 sc1\n"
    "buffer_load_dwordx4 v[220:223], v145, s[32:35], s60 offen offset:0 sc1\n"
    "buffer_load_dwordx4 v[228:231], v145, s[32:35], s60 offen offset:1024 sc1\n"
    "s_waitcnt lgkmcnt(0)\n"
    "s_waitcnt vmcnt(6)\n"
    "v_mfma_f32_16x16x32_bf16 v[160:163], v[24:27], v[200:203], 0\n"
    "s_add_u32 s60, s60, 2048\n"
    "s_cmp_eq_u32 s60, 24576\n"
    "s_cselect_b32 s60, 0, s60\n"
    "v_mfma_f32_16x16x32_bf16 v[160:163], v[44:47], v[208:211], v[160:163]\n"
    "buffer_load_dwordx4 v[232:235], v144, s[32:35], s60 offen offset:0 sc1\n"
    "v_mfma_f32_16x16x32_bf16 v[164:167], v[28:31], v[200:203], 0\n"
    "v_mfma_f32_16x16x32_bf16 v[164:167], v[48:51], v[208:211], v[164:167]\n"
    "v_mfma_f32_16x16x32_bf16 v[168:171], v[32:35], v[200:203], 0\n"
    "v_mfma_f32_16x16x32_bf16 v[168:171], v[52:55], v[208:211], v[168:171]\n"
    "v_mfma_f32_16x16x32_bf16 v[172:175], v[36:39], v[200:203], 0\n"
    "buffer_load_dwordx4 v[240:243], v144, s[32:35], s60 offen offset:1024 sc1\n"
    "v_mfma_f32_16x16x32_bf16 v[172:175], v[56:59], v[208:211], v[172:175]\n"
    "v_mfma_f32_16x16x32_bf16 v[176:179], v[40:43], v[200:203], 0\n"
    "v_mfma_f32_16x16x32_bf16 v[176:179], v[60:63], v[208:211], v[176:179]\n"
    "s_waitcnt vmcnt(6)\n"
    "v_mfma_f32_16x16x32_bf16 v[180:183], v[24:27], v[204:207], 0\n"
    "v_mfma_f32_16x16x32_bf16 v[180:183], v[44:47], v[212:215], v[180:183]\n"
    "buffer_load_dwordx4 v[236:239], v145, s[32:35], s60 offen offset:0 sc1\n"
    "v_mfma_f32_16x16x32_bf16 v[184:187], v[28:31], v[204:207], 0\n"
    "v_mfma_f32_16x16x32_bf16 v[184:187], v[48:51], v[212:215], v[184:187]\n"
    "v_mfma_f32_16x16x32_bf16 v[188:191], v[32:35], v[204:207], 0\n"
    "v_mfma_f32_16x16x32_bf16 v[188:191], v[52:55], v[212:215], v[188:191]\n"
    "ds_read_b32 v19, v151 offset:32\n"
    "v_mfma_f32_16x16x32_bf16 v[192:195], v[36:39], v[204:207], 0\n"
    "buffer_load_dwordx4 v[244:247], v145, s[32:35], s60 offen offset:1024 sc1\n"
    "v_mfma_f32_16x16x32_bf16 v[192:195], v[56:59], v[212:215], v[192:195]\n"
    "v_mfma_f32_16x16x32_bf16 v[196:199], v[40:43], v[204:207], 0\n"
    "v_mfma_f32_16x16x32_bf16 v[196:199], v[60:63], v[212:215], v[196:199]\n"
    "s_waitcnt vmcnt(6)\n"
    "v_mfma_f32_16x16x32_bf16 v[160:163], v[64:67], v[216:219], v[160:163]\n"
    "s_waitcnt lgkmcnt(0)\n"
    "v_cmp_gt_u32 vcc, 5, v19\n"
    "s_add_u32 s60, s60, 2048\n"
    "s_cmp_eq_u32 s60, 24576\n"
    "s_cselect_b32 s60, 0, s60\n"
    "v_mfma_f32_16x16x32_bf16 v[160:163], v[84:87], v[224:227], v[160:163]\n"
    "s_cbranch_vccnz Lsl21_%=\n"
    "Lbk22_%=:\n"
    "buffer_load_dwordx4 v[200:203], v144, s[32:35], s60 offen offset:0 sc1\n"
    "v_mfma_f32_16x16x32_bf16 v[164:167], v[68:71], v[216:219], v[164:167]\n"
    "ds_read_b128 v[24:27], v148 offset:0\n"
    "v_mfma_f32_16x16x32_bf16 v[164:167], v[88:91], v[224:227], v[164:167]\n"
    "ds_read_b128 v[44:47], v148 offset:10240\n"
    "v_mfma_f32_16x16x32_bf16 v[168:171], v[72:75], v[216:219], v[168:171]\n"
    "ds_read_b128 v[28:31], v148 offset:1024\n"
    "v_mfma_f32_16x16x32_bf16 v[168:171], v[92:95], v[224:227], v[168:171]\n"
    "ds_read_b128 v[48:51], v148 offset:11264\n"
    "v_mfma_f32_16x16x32_bf16 v[172:175], v[76:79], v[216:219], v[172:175]\n"
    "ds_read_b128 v[32:35], v148 offset:2048\n"
    "buffer_load_dwordx4 v[208:211], v144, s[32:35], s60 offen offset:1024 sc1\n"
    "v_mfma_f32_16x16x32_bf16 v[172:175], v[96:99], v[224:227], v[172:175]\n"
    "v_mfma_f32_16x16x32_bf16 v[176:179], v[80:83], v[216:219], v[176:179]\n"
    "ds_read_b128 v[52:55], v148 offset:12288\n"
    "v_mfma_f32_16x16x32_bf16 v[176:179], v[100:103], v[224:227], v[176:179]\n"
    "ds_read_b128 v[36:39], v148 offset:3072\n"
    "s_waitcnt vmcnt(6)\n"
    "v_mfma_f32_16x16x32_bf16 v[180:183], v[64:67], v[220:223], v[180:183]\n"
    "ds_read_b128 v[56:59], v148 offset:13312\n"
    "v_mfma_f32_16x16x32_bf16 v[180:183], v[84:87], v[228:231], v[180:183]\n"
    "ds_read_b128 v[40:43], v148 offset:4096\n"
    "buffer_load_dwordx4 v[204:207], v145, s[32:35], s60 offen offset:0 sc1\n"
    "v_mfma_f32_16x16x32_bf16 v[184:187], v[68:71], v[220:223], v[184:187]\n"
    "ds_read_b128 v[60:63], v148 offset:14336\n"
    "v_mfma_f32_16x16x32_bf16 v[184:187], v[88:91], v[228:231], v[184:187]\n"
    "v_mfma_f32_16x16x32_bf16 v[188:191], v[72:75], v[220:223], v[188:191]\n"
    "v_mfma_f32_16x16x32_bf16 v[188:191], v[92:95], v[228:231], v[188:191]\n"
    "v_mfma_f32_16x16x32_bf16 v[192:195], v[76:79], v[220:223], v[192:195]\n"
    "buffer_load_dwordx4 v[212:215], v145, s[32:35], s60 offen offset:1024 sc1\n"
    "v_mfma_f32_16x16x32_bf16 v[192:195], v[96:99], v[228:231], v[192:195]\n"
    "v_mov_b32 v18, 4\n"
    "v_mfma_f32_16x16x32_bf16 v[196:199], v[80:83], v[220:223], v[196:199]\n"
    "ds_write_b32 v17, v18 offset:48\n"
    "v_mfma_f32_16x16x32_bf16 v[196:199], v[100:103], v[228:231], v[196:199]\n"
    "s_waitcnt vmcnt(6)\n"
    "v_mfma_f32_16x16x32_bf16 v[160:163], v[104:107], v[232:235], v[160:163]\n"
    "s_add_u32 s60, s60, 2048\n"
    "s_cmp_eq_u32 s60, 24576\n"
    "s_cselect_b32 s60, 0, s60\n"
    "v_mfma_f32_16x16x32_bf16 v[160:163], v[124:127], v[240:243], v[160:163]\n"
    "buffer_load_dwordx4 v[216:219], v144, s[32:35], s60 offen offset:0 sc1\n"
    "v_mfma_f32_16x16x32_bf16 v[164:167], v[108:111], v[232:235], v[164:167]\n"
    "ds_read_b128 v[64:67], v148 offset:20480\n"
    "v_mfma_f32_16x16x32_bf16 v[164:167], v[128:131], v[240:243], v[164:167]\n"
    "ds_read_b128 v[84:87], v148 offset:30720\n"
    "v_mfma_f32_16x16x32_bf16 v[168:171], v[112:115], v[232:235], v[168:171]\n"
    "ds_read_b128 v[68:71], v148 offset:21504\n"
    "v_mfma_f32_16x16x32_bf16 v[168:171], v[132:135], v[240:243], v[168:171]\n"
    "ds_read_b128 v[88:91], v148 offset:31744\n"
    "v_mfma_f32_16x16x32_bf16 v[172:175], v[116:119], v[232:235], v[172:175]\n"
    "ds_read_b128 v[72:75], v148 offset:22528\n"
    "buffer_load_dwordx4 v[224:227], v144, s[32:35], s60 offen offset:1024 sc1\n"
    "v_mfma_f32_16x16x32_bf16 v[172:175], v[136:139], v[240:243], v[172:175]\n"
    "v_mfma_f32_16x16x32_bf16 v[176:179], v[120:123], v[232:235], v[176:179]\n"
    "ds_read_b128 v[92:95], v148 offset:32768\n"
    "v_mfma_f32_16x16x32_bf16 v[176:179], v[140:143], v[240:243], v[176:179]\n"
    "ds_read_b128 v[76:79], v148 offset:23552\n"
    "s_waitcnt vmcnt(6)\n"
    "v_mfma_f32_16x16x32_bf16 v[180:183], v[104:107], v[236:239], v[180:183]\n"
    "ds_read_b128 v[96:99], v148 offset:33792\n"
    "v_mfma_f32_16x16x32_bf16 v[180:183], v[124:127], v[244:247], v[180:183]\n"
    "ds_read_b128 v[80:83], v148 offset:24576\n"
    "buffer_load_dwordx4 v[220:223], v145, s[32:35], s60 offen offset:0 sc1\n"
    "v_mfma_f32_16x16x32_bf16 v[184:187], v[108:111], v[236:239], v[184:187]\n"
    "ds_read_b128 v[100:103], v148 offset:34816\n"
    "v_mfma_f32_16x16x32_bf16 v[184:187], v[128:131], v[244:247], v[184:187]\n"
    "v_mfma_f32_16x16x32_bf16 v[188:191], v[112:115], v[236:239], v[188:191]\n"
    "v_mfma_f32_16x16x32_bf16 v[188:191], v[132:135], v[244:247], v[188:191]\n"
    "ds_read_b32 v19, v151 offset:32\n"
    "v_mfma_f32_16x16x32_bf16 v[192:195], v[116:119], v[236:239], v[192:195]\n"
    "buffer_load_dwordx4 v[228:231], v145, s[32:35], s60 offen offset:1024 sc1\n"
    "v_mfma_f32_16x16x32_bf16 v[192:195], v[136:139], v[244:247], v[192:195]\n"
    "v_mov_b32 v18, 5\n"
    "v_mfma_f32_16x16x32_bf16 v[196:199], v[120:123], v[236:239], v[196:199]\n"
    "ds_write_b32 v17, v18 offset:48\n"
    "v_mfma_f32_16x16x32_bf16 v[196:199], v[140:143], v[244:247], v[196:199]\n"
    "s_waitcnt vmcnt(6)\n"
    "s_waitcnt lgkmcnt(15)\n"
    "v_mfma_f32_16x16x32_bf16 v[160:163], v[24:27], v[200:203], v[160:163]\n"
    "s_waitcnt lgkmcnt(1)\n"
    "v_cmp_gt_u32 vcc, 7, v19\n"
    "s_add_u32 s60, s60, 2048\n"
    "s_cmp_eq_u32 s60, 24576\n"
    "s_cselect_b32 s60, 0, s60\n"
    "v_mfma_f32_16x16x32_bf16 v[160:163], v[44:47], v[208:211], v[160:163]\n"
    "s_cbranch_vccnz Lsl25_%=\n"
    "Lbk26_%=:\n"
    "buffer_load_dwordx4 v[232:235], v144, s[32:35], s60 offen offset:0 sc1\n"
    "v_mfma_f32_16x16x32_bf16 v[164:167], v[28:31], v[200:203], v[164:167]\n"
    "ds_read_b128 v[104:107], v149 offset:0\n"
    "v_mfma_f32_16x16x32_bf16 v[164:167], v[48:51], v[208:211], v[164:167]\n"
    "ds_read_b128 v[124:127], v149 offset:10240\n"
    "v_mfma_f32_16x16x32_bf16 v[168:171], v[32:35], v[200:203], v[168:171]\n"
    "ds_read_b128 v[108:111], v149 offset:1024\n"
    "v_mfma_f32_16x16x32_bf16 v[168:171], v[52:55], v[208:211], v[168:171]\n"
    "ds_read_b128 v[128:131], v149 offset:11264\n"
    "v_mfma_f32_16x16x32_bf16 v[172:175], v[36:39], v[200:203], v[172:175]\n"
    "ds_read_b128 v[112:115], v149 offset:2048\n"
    "buffer_load_dwordx4 v[240:243], v144, s[32:35], s60 offen offset:1024 sc1\n"
    "v_mfma_f32_16x16x32_bf16 v[172:175], v[56:59], v[208:211], v[172:175]\n"
    "v_mfma_f32_16x16x32_bf16 v[176:179], v[40:43], v[200:203], v[176:179]\n"
    "ds_read_b128 v[132:135], v149 offset:12288\n"
    "v_mfma_f32_16x16x32_bf16 v[176:179], v[60:63], v[208:211], v[176:179]\n"
    "ds_read_b128 v[116:119], v149 offset:3072\n"
    "s_waitcnt vmcnt(6)\n"
    "v_mfma_f32_16x16x32_bf16 v[180:183], v[24:27], v[204:207], v[180:183]\n"
    "ds_read_b128 v[136:139], v149 offset:13312\n"
    "v_mfma_f32_16x16x32_bf16 v[180:183], v[44:47], v[212:215], v[180:183]\n"
    "ds_read_b128 v[120:123], v149 offset:4096\n"
    "buffer_load_dwordx4 v[236:239], v145, s[32:35], s60 offen offset:0 sc1\n"
    "v_mfma_f32_16x16x32_bf16 v[184:187], v[28:31], v[204:207], v[184:187]\n"
    "ds_read_b128 v[140:143], v149 offset:14336\n"
    "v_mfma_f32_16x16x32_bf16 v[184:187], v[48:51], v[212:215], v[184:187]\n"
    "v_mfma_f32_16x16x32_bf16 v[188:191], v[32:35], v[204:207], v[188:191]\n"
    "v_mfma_f32_16x16x32_bf16 v[188:191], v[52:55], v[212:215], v[188:191]\n"
    "v_mfma_f32_16x16x32_bf16 v[192:195], v[36:39], v[204:207], v[192:195]\n"
    "buffer_load_dwordx4 v[244:247], v145, s[32:35], s60 offen offset:1024 sc1\n"
    "v_mfma_f32_16x16x32_bf16 v[192:195], v[56:59], v[212:215], v[192:195]\n"
    "v_mfma_f32_16x16x32_bf16 v[196:199], v[40:43], v[204:207], v[196:199]\n"
    "v_mfma_f32_16x16x32_bf16 v[196:199], v[60:63], v[212:215], v[196:199]\n"
    "s_waitcnt vmcnt(6)\n"
    "v_mfma_f32_16x16x32_bf16 v[160:163], v[64:67], v[216:219], v[160:163]\n"
    "s_add_u32 s60, s60, 2048\n"
    "s_cmp_eq_u32 s60, 24576\n"
    "s_cselect_b32 s60, 0, s60\n"
    "v_mfma_f32_16x16x32_bf16 v[160:163], v[84:87], v[224:227], v[160:163]\n"
    "buffer_load_dwordx4 v[200:203], v144, s[32:35], s60 offen offset:0 sc1\n"
    "v_mfma_f32_16x16x32_bf16 v[164:167], v[68:71], v[216:219], v[164:167]\n"
    "ds_read_b128 v[24:27], v149 offset:20480\n"
    "v_mfma_f32_16x16x32_bf16 v[164:167], v[88:91], v[224:227], v[164:167]\n"
    "ds_read_b128 v[44:47], v149 offset:30720\n"
    "v_mfma_f32_16x16x32_bf16 v[168:171], v[72:75], v[216:219], v[168:171]\n"
    "ds_read_b128 v[28:31], v149 offset:21504\n"
    "v_mfma_f32_16x16x32_bf16 v[168:171], v[92:95], v[224:227], v[168:171]\n"
    "ds_read_b128 v[48:51], v149 offset:31744\n"
    "v_mfma_f32_16x16x32_bf16 v[172:175], v[76:79], v[216:219], v[172:175]\n"
    "ds_read_b128 v[32:35], v149 offset:22528\n"
    "buffer_load_dwordx4 v[208:211], v144, s[32:35], s60 offen offset:1024 sc1\n"
    "v_mfma_f32_16x16x32_bf16 v[172:175], v[96:99], v[224:227], v[172:175]\n"
    "v_mfma_f32_16x16x32_bf16 v[176:179], v[80:83], v[216:219], v[176:179]\n"
    "ds_read_b128 v[52:55], v149 offset:32768\n"
    "v_mfma_f32_16x16x32_bf16 v[176:179], v[100:103], v[224:227], v[176:179]\n"
    "ds_read_b128 v[36:39], v149 offset:23552\n"
    "s_waitcnt vmcnt(6)\n"
    "v_mfma_f32_16x16x32_bf16 v[180:183], v[64:67], v[220:223], v[180:183]\n"
    "ds_read_b128 v[56:59], v149 offset:33792\n"
    "v_mfma_f32_16x16x32_bf16 v[180:183], v[84:87], v[228:231], v[180:183]\n"
    "ds_read_b128 v[40:43], v149 offset:24576\n"
    "buffer_load_dwordx4 v[204:207], v145, s[32:35], s60 offen offset:0 sc1\n"
    "v_mfma_f32_16x16x32_bf16 v[184:187], v[68:71], v[220:223], v[184:187]\n"
    "ds_read_b128 v[60:63], v149 offset:34816\n"
    "v_mfma_f32_16x16x32_bf16 v[184:187], v[88:91], v[228:231], v[184:187]\n"
    "v_mfma_f32_16x16x32_bf16 v[188:191], v[72:75], v[220:223], v[188:191]\n"
    "v_mfma_f32_16x16x32_bf16 v[188:191], v[92:95], v[228:231], v[188:191]\n"
    "ds_read_b32 v19, v151 offset:32\n"
    "v_mfma_f32_16x16x32_bf16 v[192:195], v[76:79], v[220:223], v[192:195]\n"
    "buffer_load_dwordx4 v[212:215], v145, s[32:35], s60 offen offset:1024 sc1\n"
    "v_mfma_f32_16x16x32_bf16 v[192:195], v[96:99], v[228:231], v[192:195]\n"
    "v_mov_b32 v18, 7\n"
    "v_mfma_f32_16x16x32_bf16 v[196:199], v[80:83], v[220:223], v[196:199]\n"
    "ds_write_b32 v17, v18 offset:48\n"
    "v_mfma_f32_16x16x32_bf16 v[196:199], v[100:103], v[228:231], v[196:199]\n"
    "s_waitcnt vmcnt(6)\n"
    "s_waitcnt lgkmcnt(15)\n"
    "v_mfma_f32_16x16x32_bf16 v[160:163], v[104:107], v[232:235], v[160:163]\n"
    "s_waitcnt lgkmcnt(1)\n"
    "v_cmp_gt_u32 vcc, 10, v19\n"
    "s_add_u32 s60, s60, 2048\n"
    "s_cmp_eq_u32 s60, 24576\n"
    "s_cselect_b32 s60, 0, s60\n"
    "v_mfma_f32_16x16x32_bf16 v[160:163], v[124:127], v[240:243], v[160:163]\n"
    "s_cbranch_vccnz Lsl29_%=\n"
    "Lbk30_%=:\n"
    "buffer_load_dwordx4 v[216:219], v144, s[32:35], s60 offen offset:0 sc1\n"
    "v_mfma_f32_16x16x32_bf16 v[164:167], v[108:111], v[232:235], v[164:167]\n"
    "ds_read_b128 v[64:67], v146 offset:0\n"
    "v_mfma_f32_16x16x32_bf16 v[164:167], v[128:131], v[240:243], v[164:167]\n"
    "ds_read_b128 v[84:87], v146 offset:10240\n"
    "v_mfma_f32_16x16x32_bf16 v[168:171], v[112:115], v[232:235], v[168:171]\n"
    "ds_read_b128 v[68:71], v146 offset:1024\n"
    "v_mfma_f32_16x16x32_bf16 v[168:171], v[132:135], v[240:243], v[168:171]\n"
    "ds_read_b128 v[88:91], v146 offset:11264\n"
    "v_mfma_f32_16x16x32_bf16 v[172:175], v[116:119], v[232:235], v[172:175]\n"
    "ds_read_b128 v[72:75], v146 offset:2048\n"
    "buffer_load_dwordx4 v[224:227], v144, s[32:35], s60 offen offset:1024 sc1\n"
    "v_mfma_f32_16x16x32_bf16 v[172:175], v[136:139], v[240:243], v[172:175]\n"
    "v_mfma_f32_16x16x32_bf16 v[176:179], v[120:123], v[232:235], v[176:179]\n"
    "ds_read_b128 v[92:95], v146 offset:12288\n"
    "v_mfma_f32_16x16x32_bf16 v[176:179], v[140:143], v[240:243], v[176:179]\n"
    "ds_read_b128 v[76:79], v146 offset:3072\n"
    "s_waitcnt vmcnt(6)\n"
    "v_mfma_f32_16x16x32_bf16 v[180:183], v[104:107], v[236:239], v[180:183]\n"
    "ds_read_b128 v[96:99], v146 offset:13312\n"
    "v_mfma_f32_16x16x32_bf16 v[180:183], v[124:127], v[244:247], v[180:183]\n"
    "ds_read_b128 v[80:83], v146 offset:4096\n"
    "buffer_load_dwordx4 v[220:223], v145, s[32:35], s60 offen offset:0 sc1\n"
    "v_mfma_f32_16x16x32_bf16 v[184:187], v[108:111], v[236:239], v[184:187]\n"
    "ds_read_b128 v[100:103], v146 offset:14336\n"
    "v_mfma_f32_16x16x32_bf16 v[184:187], v[128:131], v[244:247], v[184:187]\n"
    "v_mfma_f32_16x16x32_bf16 v[188:191], v[112:115], v[236:239], v[188:191]\n"
    "v_mfma_f32_16x16x32_bf16 v[188:191], v[132:135], v[244:247], v[188:191]\n"
    "v_mfma_f32_16x16x32_bf16 v[192:195], v[116:119], v[236:239], v[192:195]\n"
    "buffer_load_dwordx4 v[228:231], v145, s[32:35], s60 offen offset:1024 sc1\n"
    "v_mfma_f32_16x16x32_bf16 v[192:195], v[136:139], v[244:247], v[192:195]\n"
    "v_mfma_f32_16x16x32_bf16 v[196:199], v[120:123], v[236:239], v[196:199]\n"
    "v_mfma_f32_16x16x32_bf16 v[196:199], v[140:143], v[244:247], v[196:199]\n"
    "s_waitcnt vmcnt(6)\n"
    "v_mfma_f32_16x16x32_bf16 v[160:163], v[24:27], v[200:203], v[160:163]\n"
    "s_add_u32 s60, s60, 2048\n"
    "s_cmp_eq_u32 s60, 24576\n"
    "s_cselect_b32 s60, 0, s60\n"
    "v_mfma_f32_16x16x32_bf16 v[160:163], v[44:47], v[208:211], v[160:163]\n"
    "buffer_load_dwordx4 v[232:235], v144, s[32:35], s60 offen offset:0 sc1\n"
    "v_mfma_f32_16x16x32_bf16 v[164:167], v[28:31], v[200:203], v[164:167]\n"
    "ds_read_b128 v[104:107], v147 offset:0\n"
    "v_mfma_f32_16x16x32_bf16 v[164:167], v[48:51], v[208:211], v[164:167]\n"
    "ds_read_b128 v[124:127], v147 offset:10240\n"
    "v_mfma_f32_16x16x32_bf16 v[168:171], v[32:35], v[200:203], v[168:171]\n"
    "ds_read_b128 v[108:111], v147 offset:1024\n"
    "v_mfma_f32_16x16x32_bf16 v[168:171], v[52:55], v[208:211], v[168:171]\n"
    "ds_read_b128 v[128:131], v147 offset:11264\n"
    "v_mfma_f32_16x16x32_bf16 v[172:175], v[36:39], v[200:203], v[172:175]\n"
    "ds_read_b128 v[112:115], v147 offset:2048\n"
    "buffer_load_dwordx4 v[240:243], v144, s[32:35], s60 offen offset:1024 sc1\n"
    "v_mfma_f32_16x16x32_bf16 v[172:175], v[56:59], v[208:211], v[172:175]\n"
    "v_mfma_f32_16x16x32_bf16 v[176:179], v[40:43], v[200:203], v[176:179]\n"
    "ds_read_b128 v[132:135], v147 offset:12288\n"
    "v_mfma_f32_16x16x32_bf16 v[176:179], v[60:63], v[208:211], v[176:179]\n"
    "ds_read_b128 v[116:119], v147 offset:3072\n"
    "s_waitcnt vmcnt(6)\n"
    "v_mfma_f32_16x16x32_bf16 v[180:183], v[24:27], v[204:207], v[180:183]\n"
    "ds_read_b128 v[136:139], v147 offset:13312\n"
    "v_mfma_f32_16x16x32_bf16 v[180:183], v[44:47], v[212:215], v[180:183]\n"
    "ds_read_b128 v[120:123], v147 offset:4096\n"
    "buffer_load_dwordx4 v[236:239], v145, s[32:35], s60 offen offset:0 sc1\n"
    "v_mfma_f32_16x16x32_bf16 v[184:187], v[28:31], v[204:207], v[184:187]\n"
    "ds_read_b128 v[140:143], v147 offset:14336\n"
    "v_mfma_f32_16x16x32_bf16 v[184:187], v[48:51], v[212:215], v[184:187]\n"
    "v_mfma_f32_16x16x32_bf16 v[188:191], v[32:35], v[204:207], v[188:191]\n"
    "v_mfma_f32_16x16x32_bf16 v[188:191], v[52:55], v[212:215], v[188:191]\n"
    "v_mfma_f32_16x16x32_bf16 v[192:195], v[36:39], v[204:207], v[192:195]\n"
    "buffer_load_dwordx4 v[244:247], v145, s[32:35], s60 offen offset:1024 sc1\n"
    "v_mfma_f32_16x16x32_bf16 v[192:195], v[56:59], v[212:215], v[192:195]\n"
    "v_mfma_f32_16x16x32_bf16 v[196:199], v[40:43], v[204:207], v[196:199]\n"
    "v_mfma_f32_16x16x32_bf16 v[196:199], v[60:63], v[212:215], v[196:199]\n"
    "s_waitcnt vmcnt(6)\n"
    "s_waitcnt lgkmcnt(15)\n"
    "v_mfma_f32_16x16x32_bf16 v[160:163], v[64:67], v[216:219], v[160:163]\n"
    "s_add_u32 s60, s60, 2048\n"
    "s_cmp_eq_u32 s60, 24576\n"
    "s_cselect_b32 s60, 0, s60\n"
    "v_mfma_f32_16x16x32_bf16 v[160:163], v[84:87], v[224:227], v[160:163]\n"
    "buffer_load_dwordx4 v[200:203], v144, s[32:35], s60 offen offset:0 sc1\n"
    "v_mfma_f32_16x16x32_bf16 v[164:167], v[68:71], v[216:219], v[164:167]\n"
    "ds_read_b128 v[24:27], v147 offset:20480\n"
    "v_mfma_f32_16x16x32_bf16 v[164:167], v[88:91], v[224:227], v[164:167]\n"
    "ds_read_b128 v[44:47], v147 offset:30720\n"
    "v_mfma_f32_16x16x32_bf16 v[168:171], v[72:75], v[216:219], v[168:171]\n"
    "ds_read_b128 v[28:31], v147 offset:21504\n"
    "s_waitcnt lgkmcnt(15)\n"
    "v_mfma_f32_16x16x32_bf16 v[168:171], v[92:95], v[224:227], v[168:171]\n"
    "ds_read_b128 v[48:51], v147 offset:31744\n"
    "v_mfma_f32_16x16x32_bf16 v[172:175], v[76:79], v[216:219], v[172:175]\n"
    "ds_read_b128 v[32:35], v147 offset:22528\n"
    "buffer_load_dwordx4 v[208:211], v144, s[32:35], s60 offen offset:1024 sc1\n"
    "v_mfma_f32_16x16x32_bf16 v[172:175], v[96:99], v[224:227], v[172:175]\n"
    "s_waitcnt lgkmcnt(15)\n"
    "v_mfma_f32_16x16x32_bf16 v[176:179], v[80:83], v[216:219], v[176:179]\n"
    "ds_read_b128 v[52:55], v147 offset:32768\n"
    "v_mfma_f32_16x16x32_bf16 v[176:179], v[100:103], v[224:227], v[176:179]\n"
    "ds_read_b128 v[36:39], v147 offset:23552\n"
    "s_waitcnt vmcnt(6)\n"
    "v_mfma_f32_16x16x32_bf16 v[180:183], v[64:67], v[220:223], v[180:183]\n"
    "ds_read_b128 v[56:59], v147 offset:33792\n"
    "v_mfma_f32_16x16x32_bf16 v[180:183], v[84:87], v[228:231], v[180:183]\n"
    "ds_read_b128 v[40:43], v147 offset:24576\n"
    "buffer_load_dwordx4 v[204:207], v145, s[32:35], s60 offen offset:0 sc1\n"
    "v_mfma_f32_16x16x32_bf16 v[184:187], v[68:71], v[220:223], v[184:187]\n"
    "ds_read_b128 v[60:63], v147 offset:34816\n"
    "v_mfma_f32_16x16x32_bf16 v[184:187], v[88:91], v[228:231], v[184:187]\n"
    "v_mfma_f32_16x16x32_bf16 v[188:191], v[72:75], v[220:223], v[188:191]\n"
    "v_mfma_f32_16x16x32_bf16 v[188:191], v[92:95], v[228:231], v[188:191]\n"
    "ds_read_b32 v19, v151 offset:32\n"
    "v_mfma_f32_16x16x32_bf16 v[192:195], v[76:79], v[220:223], v[192:195]\n"
    "buffer_load_dwordx4 v[212:215], v145, s[32:35], s60 offen offset:1024 sc1\n"
    "v_mfma_f32_16x16x32_bf16 v[192:195], v[96:99], v[228:231], v[192:195]\n"
    "v_mfma_f32_16x16x32_bf16 v[196:199], v[80:83], v[220:223], v[196:199]\n"
    "v_mfma_f32_16x16x32_bf16 v[196:199], v[100:103], v[228:231], v[196:199]\n"
    "s_waitcnt vmcnt(6)\n"
    "s_waitcnt lgkmcnt(15)\n"
    "v_mfma_f32_16x16x32_bf16 v[160:163], v[104:107], v[232:235], v[160:163]\n"
    "s_waitcnt lgkmcnt(0)\n"
    "v_cmp_gt_u32 vcc, 11, v19\n"
    "s_add_u32 s60, s60, 2048\n"
    "s_cmp_eq_u32 s60, 24576\n"
    "s_cselect_b32 s60, 0, s60\n"
    "v_mfma_f32_16x16x32_bf16 v[160:163], v[124:127], v[240:243], v[160:163]\n"
    "s_cbranch_vccnz Lsl33_%=\n"
    "Lbk34_%=:\n"
    "buffer_load_dwordx4 v[216:219], v144, s[32:35], s60 offen offset:0 sc1\n"
    "v_mfma_f32_16x16x32_bf16 v[164:167], v[108:111], v[232:235], v[164:167]\n"
    "ds_read_b128 v[64:67], v148 offset:0\n"
    "v_mfma_f32_16x16x32_bf16 v[164:167], v[128:131], v[240:243], v[164:167]\n"
    "ds_read_b128 v[84:87], v148 offset:10240\n"
    "v_mfma_f32_16x16x32_bf16 v[168:171], v[112:115], v[232:235], v[168:171]\n"
    "ds_read_b128 v[68:71], v148 offset:1024\n"
    "v_mfma_f32_16x16x32_bf16 v[168:171], v[132:135], v[240:243], v[168:171]\n"
    "ds_read_b128 v[88:91], v148 offset:11264\n"
    "v_mfma_f32_16x16x32_bf16 v[172:175], v[116:119], v[232:235], v[172:175]\n"
    "ds_read_b128 v[72:75], v148 offset:2048\n"
    "buffer_load_dwordx4 v[224:227], v144, s[32:35], s60 offen offset:1024 sc1\n"
    "v_mfma_f32_16x16x32_bf16 v[172:175], v[136:139], v[240:243], v[172:175]\n"
    "v_mfma_f32_16x16x32_bf16 v[176:179], v[120:123], v[232:235], v[176:179]\n"
    "ds_read_b128 v[92:95], v148 offset:12288\n"
    "v_mfma_f32_16x16x32_bf16 v[176:179], v[140:143], v[240:243], v[176:179]\n"
    "ds_read_b128 v[76:79], v148 offset:3072\n"
    "s_waitcnt vmcnt(6)\n"
    "v_mfma_f32_16x16x32_bf16 v[180:183], v[104:107], v[236:239], v[180:183]\n"
    "ds_read_b128 v[96:99], v148 offset:13312\n"
    "v_mfma_f32_16x16x32_bf16 v[180:183], v[124:127], v[244:247], v[180:183]\n"
    "ds_read_b128 v[80:83], v148 offset:4096\n"
    "buffer_load_dwordx4 v[220:223], v145, s[32:35], s60 offen offset:0 sc1\n"
    "v_mfma_f32_16x16x32_bf16 v[184:187], v[108:111], v[236:239], v[184:187]\n"
    "ds_read_b128 v[100:103], v148 offset:14336\n"
    "v_mfma_f32_16x16x32_bf16 v[184:187], v[128:131], v[244:247], v[184:187]\n"
    "v_mfma_f32_16x16x32_bf16 v[188:191], v[112:115], v[236:239], v[188:191]\n"
    "v_mfma_f32_16x16x32_bf16 v[188:191], v[132:135], v[244:247], v[188:191]\n"
    "ds_read_b32 v19, v151 offset:32\n"
    "v_mfma_f32_16x16x32_bf16 v[192:195], v[116:119], v[236:239], v[192:195]\n"
    "buffer_load_dwordx4 v[228:231], v145, s[32:35], s60 offen offset:1024 sc1\n"
    "v_mfma_f32_16x16x32_bf16 v[192:195], v[136:139], v[244:247], v[192:195]\n"
    "v_mfma_f32_16x16x32_bf16 v[196:199], v[120:123], v[236:239], v[196:199]\n"
    "v_mfma_f32_16x16x32_bf16 v[196:199], v[140:143], v[244:247], v[196:199]\n"
    "s_waitcnt vmcnt(6)\n"
    "v_mfma_f32_16x16x32_bf16 v[160:163], v[24:27], v[200:203], v[160:163]\n"
    "s_waitcnt lgkmcnt(0)\n"
    "v_cmp_gt_u32 vcc, 12, v19\n"
    "s_add_u32 s60, s60, 2048\n"
    "s_cmp_eq_u32 s60, 24576\n"
    "s_cselect_b32 s60, 0, s60\n"
    "v_mfma_f32_16x16x32_bf16 v[160:163], v[44:47], v[208:211], v[160:163]\n"
    "s_cbranch_vccnz Lsl37_%=\n"
    "Lbk38_%=:\n"
    "buffer_load_dwordx4 v[232:235], v144, s[32:35], s60 offen offset:0 sc1\n"
    "v_mfma_f32_16x16x32_bf16 v[164:167], v[28:31], v[200:203], v[164:167]\n"
    "ds_read_b128 v[104:107], v148 offset:20480\n"
    "v_mfma_f32_16x16x32_bf16 v[164:167], v[48:51], v[208:211], v[164:167]\n"
    "ds_read_b128 v[124:127], v148 offset:30720\n"
    "v_mfma_f32_16x16x32_bf16 v[168:171], v[32:35], v[200:203], v[168:171]\n"
    "ds_read_b128 v[108:111], v148 offset:21504\n"
    "v_mfma_f32_16x16x32_bf16 v[168:171], v[52:55], v[208:211], v[168:171]\n"
    "ds_read_b128 v[128:131], v148 offset:31744\n"
    "v_mfma_f32_16x16x32_bf16 v[172:175], v[36:39], v[200:203], v[172:175]\n"
    "ds_read_b128 v[112:115], v148 offset:22528\n"
    "buffer_load_dwordx4 v[240:243], v144, s[32:35], s60 offen offset:1024 sc1\n"
    "v_mfma_f32_16x16x32_bf16 v[172:175], v[56:59], v[208:211], v[172:175]\n"
    "v_mfma_f32_16x16x32_bf16 v[176:179], v[40:43], v[200:203], v[176:179]\n"
    "ds_read_b128 v[132:135], v148 offset:32768\n"
    "v_mfma_f32_16x16x32_bf16 v[176:179], v[60:63], v[208:211], v[176:179]\n"
    "ds_read_b128 v[116:119], v148 offset:23552\n"
    "s_waitcnt vmcnt(6)\n"
    "v_mfma_f32_16x16x32_bf16 v[180:183], v[24:27], v[204:207], v[180:183]\n"
    "ds_read_b128 v[136:139], v148 offset:33792\n"
    "v_mfma_f32_16x16x32_bf16 v[180:183], v[44:47], v[212:215], v[180:183]\n"
    "ds_read_b128 v[120:123], v148 offset:24576\n"
    "buffer_load_dwordx4 v[236:239], v145, s[32:35], s60 offen offset:0 sc1\n"
    "v_mfma_f32_16x16x32_bf16 v[184:187], v[28:31], v[204:207], v[184:187]\n"
    "ds_read_b128 v[140:143], v148 offset:34816\n"
    "v_mfma_f32_16x16x32_bf16 v[184:187], v[48:51], v[212:215], v[184:187]\n"
    "v_mfma_f32_16x16x32_bf16 v[188:191], v[32:35], v[204:207], v[188:191]\n"
    "v_mfma_f32_16x16x32_bf16 v[188:191], v[52:55], v[212:215], v[188:191]\n"
    "v_mfma_f32_16x16x32_bf16 v[192:195], v[36:39], v[204:207], v[192:195]\n"
    "buffer_load_dwordx4 v[244:247], v145, s[32:35], s60 offen offset:1024 sc1\n"
    "v_mfma_f32_16x16x32_bf16 v[192:195], v[56:59], v[212:215], v[192:195]\n"
    "v_mfma_f32_16x16x32_bf16 v[196:199], v[40:43], v[204:207], v[196:199]\n"
    "v_mfma_f32_16x16x32_bf16 v[196:199], v[60:63], v[212:215], v[196:199]\n"
    "s_waitcnt vmcnt(6)\n"
    "v_mfma_f32_16x16x32_bf16 v[160:163], v[64:67], v[216:219], v[160:163]\n"
    "v_mfma_f32_16x16x32_bf16 v[160:163], v[84:87], v[224:227], v[160:163]\n"
    "v_mfma_f32_16x16x32_bf16 v[164:167], v[68:71], v[216:219], v[164:167]\n"
    "v_mfma_f32_16x16x32_bf16 v[164:167], v[88:91], v[224:227], v[164:167]\n"
    "v_mfma_f32_16x16x32_bf16 v[168:171], v[72:75], v[216:219], v[168:171]\n"
    "v_mfma_f32_16x16x32_bf16 v[168:171], v[92:95], v[224:227], v[168:171]\n"
    "v_mfma_f32_16x16x32_bf16 v[172:175], v[76:79], v[216:219], v[172:175]\n"
    "v_mfma_f32_16x16x32_bf16 v[172:175], v[96:99], v[224:227], v[172:175]\n"
    "v_mfma_f32_16x16x32_bf16 v[176:179], v[80:83], v[216:219], v[176:179]\n"
    "v_mfma_f32_16x16x32_bf16 v[176:179], v[100:103], v[224:227], v[176:179]\n"
    "s_waitcnt vmcnt(4)\n"
    "v_mfma_f32_16x16x32_bf16 v[180:183], v[64:67], v[220:223], v[180:183]\n"
    "v_mfma_f32_16x16x32_bf16 v[180:183], v[84:87], v[228:231], v[180:183]\n"
    "v_mfma_f32_16x16x32_bf16 v[184:187], v[68:71], v[220:223], v[184:187]\n"
    "ds_read_b32 v19, v151 offset:48\n"
    "v_mfma_f32_16x16x32_bf16 v[184:187], v[88:91], v[228:231], v[184:187]\n"
    "v_mfma_f32_16x16x32_bf16 v[188:191], v[72:75], v[220:223], v[188:191]\n"
    "v_mfma_f32_16x16x32_bf16 v[188:191], v[92:95], v[228:231], v[188:191]\n"
    "v_mfma_f32_16x16x32_bf16 v[192:195], v[76:79], v[220:223], v[192:195]\n"
    "v_mfma_f32_16x16x32_bf16 v[192:195], v[96:99], v[228:231], v[192:195]\n"
    "v_mfma_f32_16x16x32_bf16 v[196:199], v[80:83], v[220:223], v[196:199]\n"
    "v_mfma_f32_16x16x32_bf16 v[196:199], v[100:103], v[228:231], v[196:199]\n"
    "s_waitcnt vmcnt(2)\n"
    "s_waitcnt lgkmcnt(10)\n"
    "v_mfma_f32_16x16x32_bf16 v[160:163], v[104:107], v[232:235], v[160:163]\n"
    "s_waitcnt lgkmcnt(0)\n"
    "v_cmp_gt_u32 vcc, 7, v19\n"
    "v_mfma_f32_16x16x32_bf16 v[160:163], v[124:127], v[240:243], v[160:163]\n"
    "s_cbranch_vccnz Lsl41_%=\n"
    "Lbk42_%=:\n"
    "v_mfma_f32_16x16x32_bf16 v[164:167], v[108:111], v[232:235], v[164:167]\n"
    "v_mov_b32 v18, 1\n"
    "v_mfma_f32_16x16x32_bf16 v[164:167], v[128:131], v[240:243], v[164:167]\n"
    "v_mfma_f32_16x16x32_bf16 v[168:171], v[112:115], v[232:235], v[168:171]\n"
    "v_mfma_f32_16x16x32_bf16 v[168:171], v[132:135], v[240:243], v[168:171]\n"
    "v_mfma_f32_16x16x32_bf16 v[172:175], v[116:119], v[232:235], v[172:175]\n"
    "v_mfma_f32_16x16x32_bf16 v[172:175], v[136:139], v[240:243], v[172:175]\n"
    "v_mfma_f32_16x16x32_bf16 v[176:179], v[120:123], v[232:235], v[176:179]\n"
    "v_mfma_f32_16x16x32_bf16 v[176:179], v[140:143], v[240:243], v[176:179]\n"
    "s_waitcnt vmcnt(0)\n"
    "v_mfma_f32_16x16x32_bf16 v[180:183], v[104:107], v[236:239], v[180:183]\n"
    "ds_write_b128 v154, v[160:163] offset:0\n"
    "v_mfma_f32_16x16x32_bf16 v[180:183], v[124:127], v[244:247], v[180:183]\n"
    "v_mfma_f32_16x16x32_bf16 v[184:187], v[108:111], v[236:239], v[184:187]\n"
    "ds_write_b128 v154, v[164:167] offset:1024\n"
    "v_mfma_f32_16x16x32_bf16 v[184:187], v[128:131], v[244:247], v[184:187]\n"
    "v_mfma_f32_16x16x32_bf16 v[188:191], v[112:115], v[236:239], v[188:191]\n"
    "ds_write_b128 v154, v[168:171] offset:2048\n"
    "v_mfma_f32_16x16x32_bf16 v[188:191], v[132:135], v[244:247], v[188:191]\n"
    "v_mfma_f32_16x16x32_bf16 v[192:195], v[116:119], v[236:239], v[192:195]\n"
    "ds_write_b128 v154, v[172:175] offset:3072\n"
    "v_mfma_f32_16x16x32_bf16 v[192:195], v[136:139], v[244:247], v[192:195]\n"
    "v_mfma_f32_16x16x32_bf16 v[196:199], v[120:123], v[236:239], v[196:199]\n"
    "ds_write_b128 v154, v[176:179] offset:4096\n"
    "ds_write_b32 v17, v18 offset:64\n"
    "v_mfma_f32_16x16x32_bf16 v[196:199], v[140:143], v[244:247], v[196:199]\n"
    "s_mov_b32 s3, 0\n"
    "Lsp45_%=:\n"
    "ds_read_b32 v19, v156 offset:64\n"
    "s_waitcnt lgkmcnt(0)\n"
    "v_cmp_gt_u32 vcc, 1, v19\n"
    "s_add_u32 s3, s3, 1\n"
    "s_cbranch_vccz Lsq46_%=\n"
    "s_cmp_lt_u32 s3, 8388608\n"
    "s_cbranch_scc1 Lsp45_%=\n"
    "Lsq46_%=:\n"
    "ds_read_b128 v[24:27], v155 offset:0\n"
    "ds_read_b128 v[28:31], v155 offset:1024\n"
    "ds_read_b128 v[32:35], v155 offset:2048\n"
    "ds_read_b128 v[36:39], v155 offset:3072\n"
    "ds_read_b128 v[40:43], v155 offset:4096\n"
    "s_waitcnt lgkmcnt(4)\n"
    "v_pk_add_f32 v[24:25], v[24:25], v[180:181]\n"
    "v_pk_add_f32 v[26:27], v[26:27], v[182:183]\n"
    "v_cvt_pk_bf16_f32 v200, v24, v25\n"
    "v_cvt_pk_bf16_f32 v201, v26, v27\n"
    "s_waitcnt lgkmcnt(3)\n"
    "v_pk_add_f32 v[28:29], v[28:29], v[184:185]\n"
    "v_pk_add_f32 v[30:31], v[30:31], v[186:187]\n"
    "v_cvt_pk_bf16_f32 v202, v28, v29\n"
    "v_cvt_pk_bf16_f32 v203, v30, v31\n"
    "s_nop 1\n"
    "v_permlane16_swap_b32 v200, v202\n"
    "v_permlane16_swap_b32 v201, v203\n"
    "buffer_store_dwordx4 v[200:203], v152, s[36:39], 0 offen offset:0\n"
    "s_waitcnt lgkmcnt(2)\n"
    "v_pk_add_f32 v[32:33], v[32:33], v[188:189]\n"
    "v_pk_add_f32 v[34:35], v[34:35], v[190:191]\n"
    "v_cvt_pk_bf16_f32 v204, v32, v33\n"
    "v_cvt_pk_bf16_f32 v205, v34, v35\n"
    "s_waitcnt lgkmcnt(1)\n"
    "v_pk_add_f32 v[36:37], v[36:37], v[192:193]\n"
    "v_pk_add_f32 v[38:39], v[38:39], v[194:195]\n"
    "v_cvt_pk_bf16_f32 v206, v36, v37\n"
    "v_cvt_pk_bf16_f32 v207, v38, v39\n"
    "s_nop 1\n"
    "v_permlane16_swap_b32 v204, v206\n"
    "v_permlane16_swap_b32 v205, v207\n"
    "buffer_store_dwordx4 v[204:207], v152, s[36:39], 0 offen offset:64\n"
    "s_waitcnt lgkmcnt(0)\n"
    "v_pk_add_f32 v[40:41], v[40:41], v[196:197]\n"
    "v_pk_add_f32 v[42:43], v[42:43], v[198:199]\n"
    "v_cvt_pk_bf16_f32 v208, v40, v41\n"
    "v_cvt_pk_bf16_f32 v209, v42, v43\n"
    "buffer_store_dwordx2 v[208:209], v153, s[36:39], 0 offen offset:128\n"
    "s_branch LEND_%=\n"
    "Lsl21_%=:\n"
    "s_mov_b32 s3, 0\n"
    "Lsp23_%=:\n"
    "ds_read_b32 v19, v151 offset:32\n"
    "s_waitcnt lgkmcnt(0)\n"
    "v_cmp_gt_u32 vcc, 5, v19\n"
    "s_add_u32 s3, s3, 1\n"
    "s_cbranch_vccz Lsq24_%=\n"
    "s_cmp_lt_u32 s3, 8388608\n"
    "s_cbranch_scc1 Lsp23_%=\n"
    "Lsq24_%=:\n"
    "s_branch Lbk22_%=\n"
    "Lsl25_%=:\n"
    "s_mov_b32 s3, 0\n"
    "Lsp27_%=:\n"
    "ds_read_b32 v19, v151 offset:32\n"
    "s_waitcnt lgkmcnt(0)\n"
    "v_cmp_gt_u32 vcc, 7, v19\n"
    "s_add_u32 s3, s3, 1\n"
    "s_cbranch_vccz Lsq28_%=\n"
    "s_cmp_lt_u32 s3, 8388608\n"
    "s_cbranch_scc1 Lsp27_%=\n"
    "Lsq28_%=:\n"
    "s_branch Lbk26_%=\n"
    "Lsl29_%=:\n"
    "s_mov_b32 s3, 0\n"
    "Lsp31_%=:\n"
    "ds_read_b32 v19, v151 offset:32\n"
    "s_waitcnt lgkmcnt(0)\n"
    "v_cmp_gt_u32 vcc, 10, v19\n"
    "s_add_u32 s3, s3, 1\n"
    "s_cbranch_vccz Lsq32_%=\n"
    "s_cmp_lt_u32 s3, 8388608\n"
    "s_cbranch_scc1 Lsp31_%=\n"
    "Lsq32_%=:\n"
    "s_branch Lbk30_%=\n"
    "Lsl33_%=:\n"
    "s_mov_b32 s3, 0\n"
    "Lsp35_%=:\n"
    "ds_read_b32 v19, v151 offset:32\n"
    "s_waitcnt lgkmcnt(0)\n"
    "v_cmp_gt_u32 vcc, 11, v19\n"
    "s_add_u32 s3, s3, 1\n"
    "s_cbranch_vccz Lsq36_%=\n"
    "s_cmp_lt_u32 s3, 8388608\n"
    "s_cbranch_scc1 Lsp35_%=\n"
    "Lsq36_%=:\n"
    "s_branch Lbk34_%=\n"
    "Lsl37_%=:\n"
    "s_mov_b32 s3, 0\n"
    "Lsp39_%=:\n"
    "ds_read_b32 v19, v151 offset:32\n"
    "s_waitcnt lgkmcnt(0)\n"
    "v_cmp_gt_u32 vcc, 12, v19\n"
    "s_add_u32 s3, s3, 1\n"
    "s_cbranch_vccz Lsq40_%=\n"
    "s_cmp_lt_u32 s3, 8388608\n"
    "s_cbranch_scc1 Lsp39_%=\n"
    "Lsq40_%=:\n"
    "s_branch Lbk38_%=\n"
    "Lsl41_%=:\n"
    "s_mov_b32 s3, 0\n"
    "Lsp43_%=:\n"
    "ds_read_b32 v19, v151 offset:48\n"
    "s_waitcnt lgkmcnt(0)\n"
    "v_cmp_gt_u32 vcc, 7, v19\n"
    "s_add_u32 s3, s3, 1\n"
    "s_cbranch_vccz Lsq44_%=\n"
    "s_cmp_lt_u32 s3, 8388608\n"
    "s_cbranch_scc1 Lsp43_%=\n"
    "Lsq44_%=:\n"
    "s_branch Lbk42_%=\n"
    "LL2_%=:\n"
    "s_waitcnt vmcnt(10)\n"
    "s_barrier\n"
    "s_lshr_b64 s[12:13], s[12:13], 4\n"
    "s_and_b32 s60, s12, 15\n"
    "s_add_u32 s60, s60, s47\n"
    "s_sub_u32 s58, s60, 10\n"
    "s_cmp_ge_u32 s60, 10\n"
    "s_cselect_b32 s60, s58, s60\n"
    "s_lshl_b32 s58, s60, 7\n"
    "s_mul_i32 s59, s60, 196608\n"
    "s_mul_i32 s61, s60, s53\n"
    "s_mul_i32 s8, s44, 2048\n"
    "s_add_u32 s8, s8, 4096\n"
    "v_mul_u32_u24 v13, 32, v0\n"
    "v_add_u32 v13, s8, v13\n"
    "buffer_load_dwordx2 v[68:69], v13, s[40:43], 0 offen sc1\n"
    "s_add_u32 m0, s96, 0\n"
    "s_nop 0\n"
    "buffer_load_dwordx4 v2, s[16:19], s58 offen offset:0 lds\n"
    "buffer_load_dwordx4 v3, s[16:19], s58 offen offset:1024 lds\n"
    "buffer_load_dwordx4 v4, s[16:19], s58 offen offset:2048 lds\n"
    "buffer_load_dwordx4 v5, s[16:19], s58 offen offset:3072 lds\n"
    "s_add_u32 m0, s51, 0\n"
    "s_nop 0\n"
    "buffer_load_dwordx4 v8, s[32:35], s61 offen offset:0 lds\n"
    "s_add_u32 m0, s52, 0\n"
    "s_nop 0\n"
    "buffer_load_dwordx4 v9, s[36:39], s61 offen offset:0 lds\n"
    "s_add_u32 m0, s97, 0\n"
    "s_nop 0\n"
    "buffer_load_dwordx4 v6, s[20:23], s59 offen offset:0 lds\n"
    "buffer_load_dwordx4 v7, s[20:23], s59 offen offset:1024 lds\n"
    "s_add_u32 m0, s50, 0\n"
    "s_nop 0\n"
    "buffer_load_dwordx4 v6, s[24:27], s59 offen offset:0 lds\n"
    "buffer_load_dwordx4 v7, s[24:27], s59 offen offset:1024 lds\n"
    "s_waitcnt vmcnt(10)\n"
    "s_mov_b32 s3, 0\n"
    "s_branch LWchk_%=\n"
    "LWpoll_%=:\n"
    "buffer_load_dwordx2 v[68:69], v13, s[40:43], 0 offen sc1\n"
    "s_waitcnt vmcnt(0)\n"
    "LWchk_%=:\n"
    "v_cmp_ne_u64 vcc, s[62:63], v[68:69]\n"
    "s_add_u32 s3, s3, 1\n"
    "s_cbranch_vccz LWgo_%=\n"
    "s_cmp_lt_u32 s3, 262144\n"
    "s_sleep 4\n"
    "s_cbranch_scc1 LWpoll_%=\n"
    "LWgo_%=:\n"
    "s_barrier\n"
    "s_add_u32 m0, s30, 123264\n"
    "s_add_u32 s31, s29, 0\n"
    "buffer_load_dwordx4 v23, s[4:7], s31 offen offset:0 sc1 lds\n"
    "buffer_load_dwordx4 v23, s[4:7], s31 offen offset:1024 sc1 lds\n"
    "buffer_load_dwordx4 v23, s[4:7], s31 offen offset:2048 sc1 lds\n"
    "buffer_load_dwordx4 v23, s[4:7], s31 offen offset:3072 sc1 lds\n"
    "s_add_u32 m0, s30, 127360\n"
    "s_nop 0\n"
    "buffer_load_dwordx4 v25, s[4:7], s31 offen sc1 lds\n"
    "s_add_u32 m0, s30, 41088\n"
    "s_add_u32 s31, s29, 10240\n"
    "buffer_load_dwordx4 v23, s[4:7], s31 offen offset:0 sc1 lds\n"
    "buffer_load_dwordx4 v23, s[4:7], s31 offen offset:1024 sc1 lds\n"
    "buffer_load_dwordx4 v23, s[4:7], s31 offen offset:2048 sc1 lds\n"
    "buffer_load_dwordx4 v23, s[4:7], s31 offen offset:3072 sc1 lds\n"
    "s_add_u32 m0, s30, 45184\n"
    "s_nop 0\n"
    "buffer_load_dwordx4 v25, s[4:7], s31 offen sc1 lds\n"
    "s_waitcnt vmcnt(10)\n"
    "s_barrier\n"
    "s_add_u32 m0, s30, 61568\n"
    "s_add_u32 s31, s29, 20480\n"
    "buffer_load_dwordx4 v23, s[4:7], s31 offen offset:0 sc1 lds\n"
    "buffer_load_dwordx4 v23, s[4:7], s31 offen offset:1024 sc1 lds\n"
    "buffer_load_dwordx4 v23, s[4:7], s31 offen offset:2048 sc1 lds\n"
    "buffer_load_dwordx4 v23, s[4:7], s31 offen offset:3072 sc1 lds\n"
    "s_add_u32 m0, s30, 65664\n"
    "s_nop 0\n"
    "buffer_load_dwordx4 v25, s[4:7], s31 offen sc1 lds\n"
    "s_add_u32 m0, s30, 82176\n"
    "s_add_u32 s31, s29, 30720\n"
    "buffer_load_dwordx4 v23, s[4:7], s31 offen offset:0 sc1 lds\n"
    "buffer_load_dwordx4 v23, s[4:7], s31 offen offset:1024 sc1 lds\n"
    "buffer_load_dwordx4 v23, s[4:7], s31 offen offset:2048 sc1 lds\n"
    "buffer_load_dwordx4 v23, s[4:7], s31 offen offset:3072 sc1 lds\n"
    "s_add_u32 m0, s30, 86272\n"
    "s_nop 0\n"
    "buffer_load_dwordx4 v25, s[4:7], s31 offen sc1 lds\n"
    "s_add_u32 m0, s30, 102656\n"
    "s_add_u32 s31, s29, 40960\n"
    "buffer_load_dwordx4 v23, s[4:7], s31 offen offset:0 sc1 lds\n"
    "buffer_load_dwordx4 v23, s[4:7], s31 offen offset:1024 sc1 lds\n"
    "buffer_load_dwordx4 v23, s[4:7], s31 offen offset:2048 sc1 lds\n"
    "buffer_load_dwordx4 v23, s[4:7], s31 offen offset:3072 sc1 lds\n"
    "s_add_u32 m0, s30, 106752\n"
    "s_nop 0\n"
    "buffer_load_dwordx4 v25, s[4:7], s31 offen sc1 lds\n"
    "s_waitcnt vmcnt(0)\n"
    "v_mov_b32 v22, 5\n"
    "ds_write_b32 v21, v22 offset:32\n"
    "s_or_b32 s8, s2, s44\n"
    "s_cmp_lg_u32 s8, 0\n"
    "s_cbranch_scc1 LLend_%=\n"
    "v_mov_b32 v68, s62\n"
    "v_mov_b32 v69, s63\n"
    "v_mov_b32 v12, 0\n"
    "s_mov_b64 exec, 1\n"
    "buffer_store_dwordx2 v[68:69], v12, s[40:43], 0 offen sc1\n"
    "s_mov_b64 exec, -1\n"
    "LLend_%=:\n"
    "s_mov_b32 s3, 0\n"
    "Lsp7_%=:\n"
    "ds_read_b32 v12, v20 offset:16\n"
    "s_waitcnt lgkmcnt(0)\n"
    "v_cmp_gt_u32 vcc, 2, v12\n"
    "s_add_u32 s3, s3, 1\n"
    "s_cbranch_vccz Lsq8_%=\n"
    "s_sleep 1\n"
    "s_cmp_lt_u32 s3, 8388608\n"
    "s_cbranch_scc1 Lsp7_%=\n"
    "Lsq8_%=:\n"
    "s_add_u32 m0, s30, 0\n"
    "s_add_u32 s31, s29, 51200\n"
    "buffer_load_dwordx4 v23, s[4:7], s31 offen offset:0 sc1 lds\n"
    "buffer_load_dwordx4 v23, s[4:7], s31 offen offset:1024 sc1 lds\n"
    "buffer_load_dwordx4 v23, s[4:7], s31 offen offset:2048 sc1 lds\n"
    "buffer_load_dwordx4 v23, s[4:7], s31 offen offset:3072 sc1 lds\n"
    "s_add_u32 m0, s30, 4096\n"
    "s_nop 0\n"
    "buffer_load_dwordx4 v25, s[4:7], s31 offen sc1 lds\n"
    "s_mov_b32 s3, 0\n"
    "Lsp9_%=:\n"
    "ds_read_b32 v12, v20 offset:16\n"
    "s_waitcnt lgkmcnt(0)\n"
    "v_cmp_gt_u32 vcc, 3, v12\n"
    "s_add_u32 s3, s3, 1\n"
    "s_cbranch_vccz Lsq10_%=\n"
    "s_sleep 1\n"
    "s_cmp_lt_u32 s3, 8388608\n"
    "s_cbranch_scc1 Lsp9_%=\n"
    "Lsq10_%=:\n"
    "s_add_u32 m0, s30, 20480\n"
    "s_add_u32 s31, s28, 0\n"
    "buffer_load_dwordx4 v23, s[4:7], s31 offen offset:0 sc1 lds\n"
    "buffer_load_dwordx4 v23, s[4:7], s31 offen offset:1024 sc1 lds\n"
    "buffer_load_dwordx4 v23, s[4:7], s31 offen offset:2048 sc1 lds\n"
    "buffer_load_dwordx4 v23, s[4:7], s31 offen offset:3072 sc1 lds\n"
    "s_add_u32 m0, s30, 24576\n"
    "s_nop 0\n"
    "buffer_load_dwordx4 v25, s[4:7], s31 offen sc1 lds\n"
    "s_mov_b32 s3, 0\n"
    "Lsp11_%=:\n"
    "ds_read_b32 v12, v14 offset:48\n"
    "s_waitcnt lgkmcnt(0)\n"
    "v_cmp_gt_u32 vcc, 3, v12\n"
    "s_add_u32 s3, s3, 1\n"
    "s_cbranch_vccz Lsq12_%=\n"
    "s_sleep 1\n"
    "s_cmp_lt_u32 s3, 8388608\n"
    "s_cbranch_scc1 Lsp11_%=\n"
    "Lsq12_%=:\n"
    "s_add_u32 m0, s30, 123264\n"
    "s_add_u32 s31, s28, 10240\n"
    "buffer_load_dwordx4 v23, s[4:7], s31 offen offset:0 sc1 lds\n"
    "buffer_load_dwordx4 v23, s[4:7], s31 offen offset:1024 sc1 lds\n"
    "buffer_load_dwordx4 v23, s[4:7], s31 offen offset:2048 sc1 lds\n"
    "buffer_load_dwordx4 v23, s[4:7], s31 offen offset:3072 sc1 lds\n"
    "s_add_u32 m0, s30, 127360\n"
    "s_nop 0\n"
    "buffer_load_dwordx4 v25, s[4:7], s31 offen sc1 lds\n"
    "s_add_u32 m0, s30, 41088\n"
    "s_add_u32 s31, s28, 20480\n"
    "buffer_load_dwordx4 v23, s[4:7], s31 offen offset:0 sc1 lds\n"
    "buffer_load_dwordx4 v23, s[4:7], s31 offen offset:1024 sc1 lds\n"
    "buffer_load_dwordx4 v23, s[4:7], s31 offen offset:2048 sc1 lds\n"
    "buffer_load_dwordx4 v23, s[4:7], s31 offen offset:3072 sc1 lds\n"
    "s_add_u32 m0, s30, 45184\n"
    "s_nop 0\n"
    "buffer_load_dwordx4 v25, s[4:7], s31 offen sc1 lds\n"
    "s_waitcnt vmcnt(10)\n"
    "v_mov_b32 v22, 7\n"
    "ds_write_b32 v21, v22 offset:32\n"
    "s_add_u32 m0, s30, 61568\n"
    "s_add_u32 s31, s28, 30720\n"
    "buffer_load_dwordx4 v23, s[4:7], s31 offen offset:0 sc1 lds\n"
    "buffer_load_dwordx4 v23, s[4:7], s31 offen offset:1024 sc1 lds\n"
    "buffer_load_dwordx4 v23, s[4:7], s31 offen offset:2048 sc1 lds\n"
    "buffer_load_dwordx4 v23, s[4:7], s31 offen offset:3072 sc1 lds\n"
    "s_add_u32 m0, s30, 65664\n"
    "s_nop 0\n"
    "buffer_load_dwordx4 v25, s[4:7], s31 offen sc1 lds\n"
    "s_add_u32 s31, s28, 51200\n"
    "buffer_load_dwordx4 v[48:51], v23, s[4:7], s31 offen offset:0 sc1\n"
    "buffer_load_dwordx4 v[52:55], v23, s[4:7], s31 offen offset:1024 sc1\n"
    "buffer_load_dwordx4 v[56:59], v23, s[4:7], s31 offen offset:2048 sc1\n"
    "buffer_load_dwordx4 v[60:63], v23, s[4:7], s31 offen offset:3072 sc1\n"
    "buffer_load_dwordx4 v[64:67], v25, s[4:7], s31 offen offset:0 sc1\n"
    "s_add_u32 s31, s28, 40960\n"
    "buffer_load_dwordx4 v[28:31], v23, s[4:7], s31 offen offset:0 sc1\n"
    "buffer_load_dwordx4 v[32:35], v23, s[4:7], s31 offen offset:1024 sc1\n"
    "buffer_load_dwordx4 v[36:39], v23, s[4:7], s31 offen offset:2048 sc1\n"
    "buffer_load_dwordx4 v[40:43], v23, s[4:7], s31 offen offset:3072 sc1\n"
    "buffer_load_dwordx4 v[44:47], v25, s[4:7], s31 offen offset:0 sc1\n"
    "s_waitcnt vmcnt(10)\n"
    "v_mov_b32 v22, 10\n"
    "ds_write_b32 v21, v22 offset:32\n"
    "s_waitcnt vmcnt(0)\n"
    "s_mov_b32 s3, 0\n"
    "Lsp13_%=:\n"
    "ds_read_b32 v12, v14 offset:48\n"
    "s_waitcnt lgkmcnt(0)\n"
    "v_cmp_gt_u32 vcc, 4, v12\n"
    "s_add_u32 s3, s3, 1\n"
    "s_cbranch_vccz Lsq14_%=\n"
    "s_sleep 1\n"
    "s_cmp_lt_u32 s3, 8388608\n"
    "s_cbranch_scc1 Lsp13_%=\n"
    "Lsq14_%=:\n"
    "ds_write_b128 v24, v[28:31] offset:0\n"
    "ds_write_b128 v24, v[32:35] offset:1024\n"
    "ds_write_b128 v24, v[36:39] offset:2048\n"
    "ds_write_b128 v24, v[40:43] offset:3072\n"
    "ds_write_b128 v24, v[44:47] offset:4096\n"
    "v_mov_b32 v22, 11\n"
    "ds_write_b32 v21, v22 offset:32\n"
    "s_mov_b32 s3, 0\n"
    "Lsp15_%=:\n"
    "ds_read_b32 v12, v14 offset:48\n"
    "s_waitcnt lgkmcnt(0)\n"
    "v_cmp_gt_u32 vcc, 5, v12\n"
    "s_add_u32 s3, s3, 1\n"
    "s_cbranch_vccz Lsq16_%=\n"
    "s_sleep 1\n"
    "s_cmp_lt_u32 s3, 8388608\n"
    "s_cbranch_scc1 Lsp15_%=\n"
    "Lsq16_%=:\n"
    "ds_write_b128 v24, v[48:51] offset:20480\n"
    "ds_write_b128 v24, v[52:55] offset:21504\n"
    "ds_write_b128 v24, v[56:59] offset:22528\n"
    "ds_write_b128 v24, v[60:63] offset:23552\n"
    "ds_write_b128 v24, v[64:67] offset:24576\n"
    "v_mov_b32 v22, 12\n"
    "ds_write_b32 v21, v22 offset:32\n"
    "LEND_%=:\n"
    : "+{v0}"(tid)
    : "{s[0:1]}"(__builtin_amdgcn_kernarg_segment_ptr()), "{s2}"(blockIdx.x), "s"((unsigned)(size_t)smem)
    : "v1", "v2", "v3", "v4", "v5", "v6", "v7", "v8", "v9", "v10", "v11", "v12", "v13", "v14", "v15", "v16", "v17", "v18", "v19", "v20", "v21", "v22", "v23", "v24", "v25", "v26", "v27", "v28", "v29", "v30", "v31", "v32", "v33", "v34", "v35", "v36", "v37", "v38", "v39", "v40", "v41", "v42", "v43", "v44", "v45", "v46", "v47", "v48", "v49", "v50", "v51", "v52", "v53", "v54", "v55", "v56", "v57", "v58", "v59", "v60", "v61", "v62", "v63", "v64", "v65", "v66", "v67", "v68", "v69", "v70", "v71", "v72", "v73", "v74", "v75", "v76", "v77", "v78", "v79", "v80", "v81", "v82", "v83", "v84", "v85", "v86", "v87", "v88", "v89", "v90", "v91", "v92", "v93", "v94", "v95", "v96", "v97", "v98", "v99", "v100", "v101", "v102", "v103", "v104", "v105", "v106", "v107", "v108", "v109", "v110", "v111", "v112", "v113", "v114", "v115", "v116", "v117", "v118", "v119", "v120", "v121", "v122", "v123", "v124", "v125", "v126", "v127", "v128", "v129", "v130", "v131", "v132", "v133", "v134", "v135", "v136", "v137", "v138", "v139", "v140", "v141", "v142", "v143", "v144", "v145", "v146", "v147", "v148", "v149", "v150", "v151", "v152", "v153", "v154", "v155", "v156", "v157", "v158", "v159", "v160", "v161", "v162", "v163", "v164", "v165", "v166", "v167", "v168", "v169", "v170", "v171", "v172", "v173", "v174", "v175", "v176", "v177", "v178", "v179", "v180", "v181", "v182", "v183", "v184", "v185", "v186", "v187", "v188", "v189", "v190", "v191", "v192", "v193", "v194", "v195", "v196", "v197", "v198", "v199", "v200", "v201", "v202", "v203", "v204", "v205", "v206", "v207", "v208", "v209", "v210", "v211", "v212", "v213", "v214", "v215", "v216", "v217", "v218", "v219", "v220", "v221", "v222", "v223", "v224", "v225", "v226", "v227", "v228", "v229", "v230", "v231", "v232", "v233", "v234", "v235", "v236", "v237", "v238", "v239", "v240", "v241", "v242", "v243", "v244", "v245", "v246", "v247", "v248", "v249", "v250", "v251", "v252", "v253", "v254", "v255", "s3", "s4", "s5", "s6", "s7", "s8", "s9", "s10", "s11", "s12", "s13", "s14", "s15", "s16", "s17", "s18", "s19", "s20", "s21", "s22", "s23", "s24", "s25", "s26", "s27", "s28", "s29", "s30", "s31", "s33", "s34", "s35", "s36", "s37", "s38", "s39", "s40", "s41", "s42", "s43", "s44", "s45", "s46", "s47", "s48", "s49", "s50", "s51", "s52", "s53", "s54", "s55", "s56", "s57", "s58", "s59", "s60", "s61", "s62", "s63", "s64", "s65", "s66", "s67", "s68", "s69", "s70", "s71", "s72", "s73", "s74", "s75", "s76", "s77", "s78", "s79", "s80", "s81", "s82", "s83", "s84", "s85", "s86", "s87", "s88", "s89", "s90", "s91", "s92", "s93", "s94", "s95", "s96", "s97", "vcc", "scc", "memory");
}

extern "C" void kernel_launch(void* const* d_in, const int* in_sizes, int n_in, void* d_out, int out_size, void* d_ws, size_t ws_size, hipStream_t stream) {
  glu_fused<<<256, 512, 0, stream>>>(d_in[0], d_in[1], d_in[2], d_in[3], d_out, d_ws);
}
```
